```python
import torch

def _softmax(x, axis=-1):
    return torch.softmax(x, dim=axis)
def _gelu(x):
    return torch.nn.functional.gelu(x, approximate="tanh")
def _silu(x):
    return torch.nn.functional.silu(x)
def _sigmoid(x):
    return torch.sigmoid(x)
def _layernorm(x, axis=-1, eps=1e-5):
    m = x.mean(dim=axis, keepdim=True)
    v = x.var(dim=axis, keepdim=True, unbiased=False)
    return (x - m) / torch.sqrt(v + eps)
def _scatter_add(tgt, idx, src, axis=0):
    return tgt.clone().index_add_(axis, idx, src)
def _segment_sum(src, seg, n):
    out = torch.zeros((n,) + src.shape[1:], dtype=src.dtype)
    return out.index_add_(0, seg, src)
def _pad(x, pw):
    flat = tuple(v for pair in reversed(pw) for v in pair)
    return torch.nn.functional.pad(x, flat)

kernel_name = 'standalone__gqa_softmax__sampling'

def setup_inputs():

    _w = (lambda a: (lambda t: t.to(torch.bfloat16) if t.is_floating_point() else t)(torch.as_tensor(a)))
    B, Hq, Hkv, Sq, Sk, D = 16, 32, 8, 1, 4096, 128
    return {
        "q": _w((lambda *s: torch.randn(*s, dtype=torch.bfloat16) * 0.8)(B, Hq, Sq, D)),
        "k": _w((lambda *s: torch.randn(*s, dtype=torch.bfloat16) * 0.8)(B, Hkv, Sk, D)),
        "v": _w((lambda *s: torch.randn(*s, dtype=torch.bfloat16) * 0.05)(B, Hkv, Sk, D)),
    }

def reference(q, k, v):
    B, Hq, Sq, D = q.shape
    Hkv = k.shape[1]
    rep = Hq // Hkv
    k = (lambda x, n, axis: torch.repeat_interleave(x, n, dim=axis))(k, rep, axis=1)
    v = (lambda x, n, axis: torch.repeat_interleave(x, n, dim=axis))(v, rep, axis=1)
    scale = D ** -0.5
    scores = torch.einsum("bhqd,bhkd->bhqk", q, k) * scale
    p = _softmax(scores, axis=-1)
    return torch.einsum("bhqk,bhkd->bhqd", p, v)
```

```cpp
#include <hip/hip_runtime.h>
#include <hip/hip_bf16.h>
#include <stdint.h>

#define NSPLIT 2
#define WAVES 8
#define SLOTS 4

constexpr int B_ = 16, HQ = 32, HKV = 8, SK = 4096, D = 128, G = HQ / HKV;
constexpr int TILE = 16;
constexpr int TILE_BYTES = TILE * D * 2;
constexpr int NT = SK / NSPLIT / WAVES / TILE;
constexpr int P = SLOTS / 2;
constexpr int RING = SLOTS * TILE_BYTES;
constexpr int LDS_BYTES = WAVES * RING;
constexpr float C2 = 0.08838834764831845f * 1.4426950408889634f;
static_assert(SLOTS % 2 == 0 && NT % P == 0 && NT >= P, "ring geometry");
static_assert(RING >= 2048 + 64 && RING <= 65536 && LDS_BYTES <= 163840, "LDS budget");
static_assert(4 * (SLOTS - 1) + 4 <= 63, "vmcnt range");
constexpr int PART = G * D + 2 * G;

typedef __attribute__((ext_vector_type(8))) __bf16 bf16x8;
typedef __attribute__((ext_vector_type(4))) __bf16 bf16x4;
typedef __attribute__((ext_vector_type(4))) short s16x4;
typedef __attribute__((ext_vector_type(4))) float f32x4;
typedef __attribute__((ext_vector_type(2))) float f32x2;
typedef __attribute__((ext_vector_type(4))) unsigned u32x4;
typedef __attribute__((ext_vector_type(2))) unsigned u32x2;

template <int N> __device__ __forceinline__ void wait_vm() { asm volatile("s_waitcnt vmcnt(%0)" ::"n"(N) : "memory"); }

__device__ __forceinline__ void dma_tile(unsigned lds, const char* src, unsigned v0, unsigned v1, unsigned v2, unsigned v3) {
  unsigned keep;
  asm volatile(
      "s_mov_b32 %0, m0\n\t"
      "s_mov_b32 m0, %1\n\t"
      "s_nop 0\n\t"
      "global_load_lds_dwordx4 %3, %2 offset:0 nt\n\t"
      "global_load_lds_dwordx4 %4, %2 offset:1024 nt\n\t"
      "global_load_lds_dwordx4 %5, %2 offset:2048 nt\n\t"
      "global_load_lds_dwordx4 %6, %2 offset:3072 nt\n\t"
      "s_mov_b32 m0, %0"
      : "=&s"(keep)
      : "s"(lds), "s"(src), "v"(v0), "v"(v1), "v"(v2), "v"(v3)
      : "memory");
}

__device__ __forceinline__ float xrow_max(float x) {
  auto s = __builtin_amdgcn_permlane16_swap(__float_as_uint(x), __float_as_uint(x), false, false);
  x = fmaxf(__uint_as_float(s[0]), __uint_as_float(s[1]));
  auto t = __builtin_amdgcn_permlane32_swap(__float_as_uint(x), __float_as_uint(x), false, false);
  return fmaxf(__uint_as_float(t[0]), __uint_as_float(t[1]));
}
__device__ __forceinline__ float xrow_sum(float x) {
  auto s = __builtin_amdgcn_permlane16_swap(__float_as_uint(x), __float_as_uint(x), false, false);
  x = __uint_as_float(s[0]) + __uint_as_float(s[1]);
  auto t = __builtin_amdgcn_permlane32_swap(__float_as_uint(x), __float_as_uint(x), false, false);
  return __uint_as_float(t[0]) + __uint_as_float(t[1]);
}

struct State {
  u32x4 q[4];
  f32x4 o[8];
  float m, l;
  unsigned ka[4];
  unsigned va[8];
  unsigned kv[4];
  unsigned vv[2];
};

template <int PP, bool ISSUE, int WK, int WV>
__device__ __forceinline__ void step(State& s, unsigned ring, const char* knext, const char* vnext) {
  constexpr int KOFF = (2 * PP) * TILE_BYTES, VOFF = (2 * PP + 1) * TILE_BYTES;
  u32x4 k0, k1, k2, k3;
  asm volatile(
      "s_waitcnt vmcnt(%8)\n\t"
      "ds_read_b128 %0, %4 offset:%9\n\t"
      "ds_read_b128 %1, %5 offset:%9\n\t"
      "ds_read_b128 %2, %6 offset:%9\n\t"
      "ds_read_b128 %3, %7 offset:%9\n\t"
      "s_waitcnt lgkmcnt(0)"
      : "=&v"(k0), "=&v"(k1), "=&v"(k2), "=&v"(k3)
      : "v"(s.ka[0]), "v"(s.ka[1]), "v"(s.ka[2]), "v"(s.ka[3]), "n"(WK), "n"(KOFF)
      : "memory");
  if constexpr (ISSUE) dma_tile(ring + KOFF, knext, s.kv[0], s.kv[1], s.kv[2], s.kv[3]);
  f32x4 sc = {0.f, 0.f, 0.f, 0.f};
  sc = __builtin_amdgcn_mfma_f32_16x16x32_bf16(__builtin_bit_cast(bf16x8, k0), __builtin_bit_cast(bf16x8, s.q[0]), sc, 0, 0, 0);
  sc = __builtin_amdgcn_mfma_f32_16x16x32_bf16(__builtin_bit_cast(bf16x8, k1), __builtin_bit_cast(bf16x8, s.q[1]), sc, 0, 0, 0);
  sc = __builtin_amdgcn_mfma_f32_16x16x32_bf16(__builtin_bit_cast(bf16x8, k2), __builtin_bit_cast(bf16x8, s.q[2]), sc, 0, 0, 0);
  sc = __builtin_amdgcn_mfma_f32_16x16x32_bf16(__builtin_bit_cast(bf16x8, k3), __builtin_bit_cast(bf16x8, s.q[3]), sc, 0, 0, 0);
  const float tmax = xrow_max(fmaxf(fmaxf(sc[0], sc[1]), fmaxf(sc[2], sc[3])));
  const float mn = fmaxf(s.m, tmax);
  if (__any(mn > s.m)) {
    const float alpha = __builtin_amdgcn_exp2f((s.m - mn) * C2);
    s.l *= alpha;
#pragma unroll
    for (int c = 0; c < 8; ++c) s.o[c] *= alpha;
    s.m = mn;
  }
  const float mc = -s.m * C2;
  const float p0 = __builtin_amdgcn_exp2f(fmaf(sc[0], C2, mc)), p1 = __builtin_amdgcn_exp2f(fmaf(sc[1], C2, mc));
  const float p2 = __builtin_amdgcn_exp2f(fmaf(sc[2], C2, mc)), p3 = __builtin_amdgcn_exp2f(fmaf(sc[3], C2, mc));
  s.l += (p0 + p1) + (p2 + p3);
  const bf16x4 pb = {(__bf16)p0, (__bf16)p1, (__bf16)p2, (__bf16)p3};
  const s16x4 pB = __builtin_bit_cast(s16x4, pb);
  u32x2 v0, v1, v2, v3, v4, v5, v6, v7;
  asm volatile(
      "s_waitcnt vmcnt(%16)\n\t"
      "ds_read_b64_tr_b16 %0, %8 offset:%17\n\t"
      "ds_read_b64_tr_b16 %1, %9 offset:%17\n\t"
      "ds_read_b64_tr_b16 %2, %10 offset:%17\n\t"
      "ds_read_b64_tr_b16 %3, %11 offset:%17\n\t"
      "ds_read_b64_tr_b16 %4, %12 offset:%17\n\t"
      "ds_read_b64_tr_b16 %5, %13 offset:%17\n\t"
      "ds_read_b64_tr_b16 %6, %14 offset:%17\n\t"
      "ds_read_b64_tr_b16 %7, %15 offset:%17\n\t"
      "s_waitcnt lgkmcnt(0)"
      : "=&v"(v0), "=&v"(v1), "=&v"(v2), "=&v"(v3), "=&v"(v4), "=&v"(v5), "=&v"(v6), "=&v"(v7)
      : "v"(s.va[0]), "v"(s.va[1]), "v"(s.va[2]), "v"(s.va[3]), "v"(s.va[4]), "v"(s.va[5]), "v"(s.va[6]), "v"(s.va[7]),
        "n"(WV), "n"(VOFF)
      : "memory");
  if constexpr (ISSUE) dma_tile(ring + VOFF, vnext, s.vv[0], s.vv[1], s.vv[0], s.vv[1]);
#define PV(c, v) s.o[c] = __builtin_amdgcn_mfma_f32_16x16x16bf16_1k(__builtin_bit_cast(s16x4, v), pB, s.o[c], 0, 0, 0)
  PV(0, v0); PV(1, v1); PV(2, v2); PV(3, v3); PV(4, v4); PV(5, v5); PV(6, v6); PV(7, v7);
#undef PV
}

template <int PP> struct Steady {
  static __device__ __forceinline__ void run(State& s, unsigned ring, const char* kn, const char* vn) {
    if constexpr (PP < P) {
      step<PP, true, 4 * (SLOTS - 1), 4 * (SLOTS - 1)>(s, ring, kn + (size_t)PP * WAVES * TILE_BYTES, vn + (size_t)PP * WAVES * TILE_BYTES);
      Steady<PP + 1>::run(s, ring, kn, vn);
    }
  }
};
template <int PP> struct Tail {
  static __device__ __forceinline__ void run(State& s, unsigned ring) {
    if constexpr (PP < P) {
      step<PP, false, 4 * (SLOTS - 1 - 2 * PP), 4 * (SLOTS - 2 - 2 * PP)>(s, ring, nullptr, nullptr);
      Tail<PP + 1>::run(s, ring);
    }
  }
};

__global__ void __launch_bounds__(WAVES * 64, WAVES / 4)
attn_split(const char* __restrict__ Q, const char* __restrict__ K, const char* __restrict__ V,
           __hip_bfloat16* __restrict__ out, float* __restrict__ ws) {
  extern __shared__ __attribute__((aligned(16))) char lds[];
  const int tid = threadIdx.x, lane = tid & 63, wave = __builtin_amdgcn_readfirstlane(tid >> 6);
  const int bh = blockIdx.x / NSPLIT, sp = blockIdx.x % NSPLIT;
  const int g = lane >> 4, t = lane & 15;
  const unsigned ring = (unsigned)(size_t)lds + wave * RING;
  State s;
#pragma unroll
  for (int i = 0; i < 4; ++i) s.kv[i] = 256 * g + 16 * (t ^ g ^ (4 * i));
#pragma unroll
  for (int i = 0; i < 2; ++i) s.vv[i] = 256 * g + 16 * (t ^ (2 * g) ^ (8 * i));
  const size_t base = ((size_t)bh * SK + (size_t)sp * (SK / NSPLIT) + (size_t)wave * TILE) * (D * 2);
  const char* kp = K + base;
  const char* vp = V + base;
  {
    const __amdgpu_buffer_rsrc_t qr = __builtin_amdgcn_make_buffer_rsrc((void*)(Q + (size_t)bh * (G * D * 2)), (short)0, G * D * 2, 0x00020000);
    const unsigned qoff = t * 256 + g * 16;
    asm volatile(
        "s_nop 4\n\t"
        "buffer_load_dwordx4 %0, %4, %5, 0 offen offset:0\n\t"
        "buffer_load_dwordx4 %1, %4, %5, 0 offen offset:64\n\t"
        "buffer_load_dwordx4 %2, %4, %5, 0 offen offset:128\n\t"
        "buffer_load_dwordx4 %3, %4, %5, 0 offen offset:192"
        : "=&v"(s.q[0]), "=&v"(s.q[1]), "=&v"(s.q[2]), "=&v"(s.q[3])
        : "v"(qoff), "s"(qr)
        : "memory");
  }
#pragma unroll
  for (int pp = 0; pp < P; ++pp) {
    dma_tile(ring + (2 * pp) * TILE_BYTES, kp + (size_t)pp * WAVES * TILE_BYTES, s.kv[0], s.kv[1], s.kv[2], s.kv[3]);
    dma_tile(ring + (2 * pp + 1) * TILE_BYTES, vp + (size_t)pp * WAVES * TILE_BYTES, s.vv[0], s.vv[1], s.vv[0], s.vv[1]);
  }
#pragma unroll
  for (int i = 0; i < 4; ++i) s.ka[i] = ring + ((256 * t + 16 * (g ^ t)) ^ (64 * i));
  {
    const int row = 4 * g + (t >> 2);
    const unsigned a0 = 256 * row + 32 * (row & 7) + 8 * (t & 3);
#pragma unroll
    for (int c = 0; c < 8; ++c) s.va[c] = ring + (a0 ^ (32 * c));
  }
#pragma unroll
  for (int c = 0; c < 8; ++c) s.o[c] = f32x4{0.f, 0.f, 0.f, 0.f};
  s.m = -INFINITY;
  s.l = 0.f;
  asm volatile("s_waitcnt vmcnt(%4)" : "+v"(s.q[0]), "+v"(s.q[1]), "+v"(s.q[2]), "+v"(s.q[3]) : "n"(4 * SLOTS) : "memory");

  kp += (size_t)P * WAVES * TILE_BYTES;
  vp += (size_t)P * WAVES * TILE_BYTES;
  for (int it = 0; it < NT / P - 1; ++it) {
    Steady<0>::run(s, ring, kp, vp);
    kp += (size_t)P * WAVES * TILE_BYTES;
    vp += (size_t)P * WAVES * TILE_BYTES;
  }
  Tail<0>::run(s, ring);

  const float lsum = xrow_sum(s.l);
  float* mine = reinterpret_cast<float*>(lds + wave * RING);
  if (t < G) {
#pragma unroll
    for (int c = 0; c < 8; ++c) *reinterpret_cast<f32x4*>(mine + t * D + 16 * c + 4 * g) = s.o[c];
    if (g == 0) { mine[G * D + t] = s.m; mine[G * D + G + t] = lsum; }
  }
  __syncthreads();
  if (tid < 256) {
    const int h = tid >> 6, d0 = 2 * lane;
    float M = -INFINITY;
#pragma unroll
    for (int w = 0; w < WAVES; ++w) M = fmaxf(M, reinterpret_cast<const float*>(lds + w * RING)[G * D + h]);
    float L = 0.f, o0 = 0.f, o1 = 0.f;
#pragma unroll
    for (int w = 0; w < WAVES; ++w) {
      const float* r = reinterpret_cast<const float*>(lds + w * RING);
      const float wt = __builtin_amdgcn_exp2f((r[G * D + h] - M) * C2);
      const f32x2 a = *reinterpret_cast<const f32x2*>(r + h * D + d0);
      L = fmaf(wt, r[G * D + G + h], L);
      o0 = fmaf(wt, a[0], o0);
      o1 = fmaf(wt, a[1], o1);
    }
    if constexpr (NSPLIT == 1) {
      const float inv = 1.f / L;
      __hip_bfloat162 r2 = __float22bfloat162_rn(make_float2(o0 * inv, o1 * inv));
      *reinterpret_cast<__hip_bfloat162*>(out + ((size_t)bh * G + h) * D + d0) = r2;
    } else {
      float* pp = ws + (size_t)blockIdx.x * PART;
      *reinterpret_cast<f32x2*>(pp + h * D + d0) = f32x2{o0, o1};
      if (lane == 0) { pp[G * D + h] = M; pp[G * D + G + h] = L; }
    }
  }
}

__global__ void __launch_bounds__(256) attn_combine(const float* __restrict__ ws, __hip_bfloat16* __restrict__ out) {
  const int bh = blockIdx.x, h = threadIdx.x >> 6, d0 = 2 * (threadIdx.x & 63);
  const float* pp = ws + (size_t)bh * NSPLIT * PART;
  float M = -INFINITY;
#pragma unroll
  for (int i = 0; i < NSPLIT; ++i) M = fmaxf(M, pp[i * PART + G * D + h]);
  float L = 0.f, o0 = 0.f, o1 = 0.f;
#pragma unroll
  for (int i = 0; i < NSPLIT; ++i) {
    const float* r = pp + i * PART;
    const float wt = __builtin_amdgcn_exp2f((r[G * D + h] - M) * C2);
    const f32x2 a = *reinterpret_cast<const f32x2*>(r + h * D + d0);
    L = fmaf(wt, r[G * D + G + h], L);
    o0 = fmaf(wt, a[0], o0);
    o1 = fmaf(wt, a[1], o1);
  }
  const float inv = 1.f / L;
  __hip_bfloat162 r2 = __float22bfloat162_rn(make_float2(o0 * inv, o1 * inv));
  *reinterpret_cast<__hip_bfloat162*>(out + ((size_t)bh * G + h) * D + d0) = r2;
}

extern "C" void kernel_launch(void* const* d_in, const int* in_sizes, int n_in, void* d_out, int out_size,
                              void* d_ws, size_t ws_size, hipStream_t stream) {
  (void)in_sizes; (void)n_in; (void)out_size; (void)ws_size;
  (void)hipFuncSetAttribute((const void*)attn_split, hipFuncAttributeMaxDynamicSharedMemorySize, LDS_BYTES);
  attn_split<<<dim3(B_ * HKV * NSPLIT), dim3(WAVES * 64), LDS_BYTES, stream>>>(
      (const char*)d_in[0], (const char*)d_in[1], (const char*)d_in[2], (__hip_bfloat16*)d_out, (float*)d_ws);
  if (NSPLIT > 1) attn_combine<<<dim3(B_ * HKV), dim3(256), 0, stream>>>((const float*)d_ws, (__hip_bfloat16*)d_out);
}
```

```cpp
#include <hip/hip_runtime.h>
#include <hip/hip_bf16.h>
#include <stdint.h>

#define NSPLIT 2
#define WAVES 8
#define SLOTS 4

constexpr int B_ = 16, HQ = 32, HKV = 8, SK = 4096, D = 128, G = HQ / HKV;
constexpr int TILE = 16;
constexpr int TILE_BYTES = TILE * D * 2;
constexpr int NT = SK / NSPLIT / WAVES / TILE;
constexpr int P = SLOTS / 2;
constexpr int RING = SLOTS * TILE_BYTES;
constexpr int LDS_BYTES = WAVES * RING;
constexpr float C2 = 0.08838834764831845f * 1.4426950408889634f;
static_assert(SLOTS % 2 == 0 && NT % P == 0 && NT >= P, "ring geometry");
static_assert(RING >= 2048 + 64 && RING <= 65536 && LDS_BYTES <= 163840, "LDS budget");
static_assert(4 * (SLOTS - 1) + 4 <= 63, "vmcnt range");
constexpr int PART = G * D + 2 * G;

typedef __attribute__((ext_vector_type(8))) __bf16 bf16x8;
typedef __attribute__((ext_vector_type(4))) __bf16 bf16x4;
typedef __attribute__((ext_vector_type(4))) short s16x4;
typedef __attribute__((ext_vector_type(4))) float f32x4;
typedef __attribute__((ext_vector_type(2))) float f32x2;
typedef __attribute__((ext_vector_type(4))) unsigned u32x4;
typedef __attribute__((ext_vector_type(2))) unsigned u32x2;

template <int N> __device__ __forceinline__ void wait_vm() { asm volatile("s_waitcnt vmcnt(%0)" ::"n"(N) : "memory"); }

#define KPOL "nt"
#define VPOL "nt"
#define DMA_TILE(POL)                                              \
  asm volatile(                                                    \
      "s_mov_b32 %0, m0\n\t"                                       \
      "s_mov_b32 m0, %1\n\t"                                       \
      "s_nop 0\n\t"                                                \
      "global_load_lds_dwordx4 %3, %2 offset:0 " POL "\n\t"        \
      "global_load_lds_dwordx4 %4, %2 offset:1024 " POL "\n\t"     \
      "global_load_lds_dwordx4 %5, %2 offset:2048 " POL "\n\t"     \
      "global_load_lds_dwordx4 %6, %2 offset:3072 " POL "\n\t"     \
      "s_mov_b32 m0, %0"                                           \
      : "=&s"(keep)                                                \
      : "s"(lds), "s"(src), "v"(v0), "v"(v1), "v"(v2), "v"(v3)     \
      : "memory")
template <bool ISV>
__device__ __forceinline__ void dma_tile(unsigned lds, const char* src, unsigned v0, unsigned v1, unsigned v2, unsigned v3) {
  unsigned keep;
  if constexpr (ISV) DMA_TILE(VPOL); else DMA_TILE(KPOL);
}

__device__ __forceinline__ float xrow_max(float x) {
  auto s = __builtin_amdgcn_permlane16_swap(__float_as_uint(x), __float_as_uint(x), false, false);
  x = fmaxf(__uint_as_float(s[0]), __uint_as_float(s[1]));
  auto t = __builtin_amdgcn_permlane32_swap(__float_as_uint(x), __float_as_uint(x), false, false);
  return fmaxf(__uint_as_float(t[0]), __uint_as_float(t[1]));
}
__device__ __forceinline__ float xrow_sum(float x) {
  auto s = __builtin_amdgcn_permlane16_swap(__float_as_uint(x), __float_as_uint(x), false, false);
  x = __uint_as_float(s[0]) + __uint_as_float(s[1]);
  auto t = __builtin_amdgcn_permlane32_swap(__float_as_uint(x), __float_as_uint(x), false, false);
  return __uint_as_float(t[0]) + __uint_as_float(t[1]);
}

struct State {
  u32x4 q[4];
  f32x4 o[8];
  float m, l;
  unsigned ka[4];
  unsigned va[8];
  unsigned kv[4];
  unsigned vv[2];
};

template <int PP, bool ISSUE, int WK, int WV>
__device__ __forceinline__ void step(State& s, unsigned ring, const char* knext, const char* vnext) {
  constexpr int KOFF = (2 * PP) * TILE_BYTES, VOFF = (2 * PP + 1) * TILE_BYTES;
  u32x4 k0, k1, k2, k3;
  asm volatile(
      "s_waitcnt vmcnt(%8)\n\t"
      "ds_read_b128 %0, %4 offset:%9\n\t"
      "ds_read_b128 %1, %5 offset:%9\n\t"
      "ds_read_b128 %2, %6 offset:%9\n\t"
      "ds_read_b128 %3, %7 offset:%9\n\t"
      "s_waitcnt lgkmcnt(0)"
      : "=&v"(k0), "=&v"(k1), "=&v"(k2), "=&v"(k3)
      : "v"(s.ka[0]), "v"(s.ka[1]), "v"(s.ka[2]), "v"(s.ka[3]), "n"(WK), "n"(KOFF)
      : "memory");
  if constexpr (ISSUE) dma_tile<false>(ring + KOFF, knext, s.kv[0], s.kv[1], s.kv[2], s.kv[3]);
  f32x4 sc = {0.f, 0.f, 0.f, 0.f};
  sc = __builtin_amdgcn_mfma_f32_16x16x32_bf16(__builtin_bit_cast(bf16x8, k0), __builtin_bit_cast(bf16x8, s.q[0]), sc, 0, 0, 0);
  sc = __builtin_amdgcn_mfma_f32_16x16x32_bf16(__builtin_bit_cast(bf16x8, k1), __builtin_bit_cast(bf16x8, s.q[1]), sc, 0, 0, 0);
  sc = __builtin_amdgcn_mfma_f32_16x16x32_bf16(__builtin_bit_cast(bf16x8, k2), __builtin_bit_cast(bf16x8, s.q[2]), sc, 0, 0, 0);
  sc = __builtin_amdgcn_mfma_f32_16x16x32_bf16(__builtin_bit_cast(bf16x8, k3), __builtin_bit_cast(bf16x8, s.q[3]), sc, 0, 0, 0);
  const float tmax = xrow_max(fmaxf(fmaxf(sc[0], sc[1]), fmaxf(sc[2], sc[3])));
  const float mn = fmaxf(s.m, tmax);
  if (__any(mn > s.m)) {
    const float alpha = __builtin_amdgcn_exp2f((s.m - mn) * C2);
    s.l *= alpha;
#pragma unroll
    for (int c = 0; c < 8; ++c) s.o[c] *= alpha;
    s.m = mn;
  }
  const float mc = -s.m * C2;
  const float p0 = __builtin_amdgcn_exp2f(fmaf(sc[0], C2, mc)), p1 = __builtin_amdgcn_exp2f(fmaf(sc[1], C2, mc));
  const float p2 = __builtin_amdgcn_exp2f(fmaf(sc[2], C2, mc)), p3 = __builtin_amdgcn_exp2f(fmaf(sc[3], C2, mc));
  s.l += (p0 + p1) + (p2 + p3);
  const bf16x4 pb = {(__bf16)p0, (__bf16)p1, (__bf16)p2, (__bf16)p3};
  const s16x4 pB = __builtin_bit_cast(s16x4, pb);
  u32x2 v0, v1, v2, v3, v4, v5, v6, v7;
  asm volatile(
      "s_waitcnt vmcnt(%16)\n\t"
      "ds_read_b64_tr_b16 %0, %8 offset:%17\n\t"
      "ds_read_b64_tr_b16 %1, %9 offset:%17\n\t"
      "ds_read_b64_tr_b16 %2, %10 offset:%17\n\t"
      "ds_read_b64_tr_b16 %3, %11 offset:%17\n\t"
      "ds_read_b64_tr_b16 %4, %12 offset:%17\n\t"
      "ds_read_b64_tr_b16 %5, %13 offset:%17\n\t"
      "ds_read_b64_tr_b16 %6, %14 offset:%17\n\t"
      "ds_read_b64_tr_b16 %7, %15 offset:%17\n\t"
      "s_waitcnt lgkmcnt(0)"
      : "=&v"(v0), "=&v"(v1), "=&v"(v2), "=&v"(v3), "=&v"(v4), "=&v"(v5), "=&v"(v6), "=&v"(v7)
      : "v"(s.va[0]), "v"(s.va[1]), "v"(s.va[2]), "v"(s.va[3]), "v"(s.va[4]), "v"(s.va[5]), "v"(s.va[6]), "v"(s.va[7]),
        "n"(WV), "n"(VOFF)
      : "memory");
  if constexpr (ISSUE) dma_tile<true>(ring + VOFF, vnext, s.vv[0], s.vv[1], s.vv[0], s.vv[1]);
#define PV(c, v) s.o[c] = __builtin_amdgcn_mfma_f32_16x16x16bf16_1k(__builtin_bit_cast(s16x4, v), pB, s.o[c], 0, 0, 0)
  PV(0, v0); PV(1, v1); PV(2, v2); PV(3, v3); PV(4, v4); PV(5, v5); PV(6, v6); PV(7, v7);
#undef PV
}

template <int PP> struct Steady {
  static __device__ __forceinline__ void run(State& s, unsigned ring, const char* kn, const char* vn) {
    if constexpr (PP < P) {
      step<PP, true, 4 * (SLOTS - 1), 4 * (SLOTS - 1)>(s, ring, kn + (size_t)PP * WAVES * TILE_BYTES, vn + (size_t)PP * WAVES * TILE_BYTES);
      Steady<PP + 1>::run(s, ring, kn, vn);
    }
  }
};
template <int PP> struct Tail {
  static __device__ __forceinline__ void run(State& s, unsigned ring) {
    if constexpr (PP < P) {
      step<PP, false, 4 * (SLOTS - 1 - 2 * PP), 4 * (SLOTS - 2 - 2 * PP)>(s, ring, nullptr, nullptr);
      Tail<PP + 1>::run(s, ring);
    }
  }
};

__global__ void __launch_bounds__(WAVES * 64, WAVES / 4)
attn_split(const char* __restrict__ Q, const char* __restrict__ K, const char* __restrict__ V,
           __hip_bfloat16* __restrict__ out, float* __restrict__ ws) {
  extern __shared__ __attribute__((aligned(16))) char lds[];
  const int tid = threadIdx.x, lane = tid & 63, wave = __builtin_amdgcn_readfirstlane(tid >> 6);
  const int bh = blockIdx.x / NSPLIT, sp = blockIdx.x % NSPLIT;
  const int g = lane >> 4, t = lane & 15;
  const unsigned ring = (unsigned)(size_t)lds + wave * RING;
  State s;
#pragma unroll
  for (int i = 0; i < 4; ++i) s.kv[i] = 256 * g + 16 * (t ^ g ^ (4 * i));
#pragma unroll
  for (int i = 0; i < 2; ++i) s.vv[i] = 256 * g + 16 * (t ^ (2 * g) ^ (8 * i));
  const size_t base = ((size_t)bh * SK + (size_t)sp * (SK / NSPLIT) + (size_t)wave * TILE) * (D * 2);
  const char* kp = K + base;
  const char* vp = V + base;
  {
    const __amdgpu_buffer_rsrc_t qr = __builtin_amdgcn_make_buffer_rsrc((void*)(Q + (size_t)bh * (G * D * 2)), (short)0, G * D * 2, 0x00020000);
    const unsigned qoff = t * 256 + g * 16;
    asm volatile(
        "s_nop 4\n\t"
        "buffer_load_dwordx4 %0, %4, %5, 0 offen offset:0\n\t"
        "buffer_load_dwordx4 %1, %4, %5, 0 offen offset:64\n\t"
        "buffer_load_dwordx4 %2, %4, %5, 0 offen offset:128\n\t"
        "buffer_load_dwordx4 %3, %4, %5, 0 offen offset:192"
        : "=&v"(s.q[0]), "=&v"(s.q[1]), "=&v"(s.q[2]), "=&v"(s.q[3])
        : "v"(qoff), "s"(qr)
        : "memory");
  }
#pragma unroll
  for (int pp = 0; pp < P; ++pp) {
    dma_tile<false>(ring + (2 * pp) * TILE_BYTES, kp + (size_t)pp * WAVES * TILE_BYTES, s.kv[0], s.kv[1], s.kv[2], s.kv[3]);
    dma_tile<true>(ring + (2 * pp + 1) * TILE_BYTES, vp + (size_t)pp * WAVES * TILE_BYTES, s.vv[0], s.vv[1], s.vv[0], s.vv[1]);
  }
#pragma unroll
  for (int i = 0; i < 4; ++i) s.ka[i] = ring + ((256 * t + 16 * (g ^ t)) ^ (64 * i));
  {
    const int row = 4 * g + (t >> 2);
    const unsigned a0 = 256 * row + 32 * (row & 7) + 8 * (t & 3);
#pragma unroll
    for (int c = 0; c < 8; ++c) s.va[c] = ring + (a0 ^ (32 * c));
  }
#pragma unroll
  for (int c = 0; c < 8; ++c) s.o[c] = f32x4{0.f, 0.f, 0.f, 0.f};
  s.m = -INFINITY;
  s.l = 0.f;
  asm volatile("s_waitcnt vmcnt(%4)" : "+v"(s.q[0]), "+v"(s.q[1]), "+v"(s.q[2]), "+v"(s.q[3]) : "n"(4 * SLOTS) : "memory");

  kp += (size_t)P * WAVES * TILE_BYTES;
  vp += (size_t)P * WAVES * TILE_BYTES;
  for (int it = 0; it < NT / P - 1; ++it) {
    Steady<0>::run(s, ring, kp, vp);
    kp += (size_t)P * WAVES * TILE_BYTES;
    vp += (size_t)P * WAVES * TILE_BYTES;
  }
  Tail<0>::run(s, ring);

  const float lsum = xrow_sum(s.l);
  float* mine = reinterpret_cast<float*>(lds + wave * RING);
  if (t < G) {
#pragma unroll
    for (int c = 0; c < 8; ++c) *reinterpret_cast<f32x4*>(mine + t * D + 16 * c + 4 * g) = s.o[c];
    if (g == 0) { mine[G * D + t] = s.m; mine[G * D + G + t] = lsum; }
  }
  __syncthreads();
  if (tid < 256) {
    const int h = tid >> 6, d0 = 2 * lane;
    float M = -INFINITY;
#pragma unroll
    for (int w = 0; w < WAVES; ++w) M = fmaxf(M, reinterpret_cast<const float*>(lds + w * RING)[G * D + h]);
    float L = 0.f, o0 = 0.f, o1 = 0.f;
#pragma unroll
    for (int w = 0; w < WAVES; ++w) {
      const float* r = reinterpret_cast<const float*>(lds + w * RING);
      const float wt = __builtin_amdgcn_exp2f((r[G * D + h] - M) * C2);
      const f32x2 a = *reinterpret_cast<const f32x2*>(r + h * D + d0);
      L = fmaf(wt, r[G * D + G + h], L);
      o0 = fmaf(wt, a[0], o0);
      o1 = fmaf(wt, a[1], o1);
    }
    if constexpr (NSPLIT == 1) {
      const float inv = 1.f / L;
      __hip_bfloat162 r2 = __float22bfloat162_rn(make_float2(o0 * inv, o1 * inv));
      *reinterpret_cast<__hip_bfloat162*>(out + ((size_t)bh * G + h) * D + d0) = r2;
    } else {
      float* pp = ws + (size_t)blockIdx.x * PART;
      *reinterpret_cast<f32x2*>(pp + h * D + d0) = f32x2{o0, o1};
      if (lane == 0) { pp[G * D + h] = M; pp[G * D + G + h] = L; }
    }
  }
}

__global__ void __launch_bounds__(256) attn_combine(const float* __restrict__ ws, __hip_bfloat16* __restrict__ out) {
  const int bh = blockIdx.x, h = threadIdx.x >> 6, d0 = 2 * (threadIdx.x & 63);
  const float* pp = ws + (size_t)bh * NSPLIT * PART;
  float M = -INFINITY;
#pragma unroll
  for (int i = 0; i < NSPLIT; ++i) M = fmaxf(M, pp[i * PART + G * D + h]);
  float L = 0.f, o0 = 0.f, o1 = 0.f;
#pragma unroll
  for (int i = 0; i < NSPLIT; ++i) {
    const float* r = pp + i * PART;
    const float wt = __builtin_amdgcn_exp2f((r[G * D + h] - M) * C2);
    const f32x2 a = *reinterpret_cast<const f32x2*>(r + h * D + d0);
    L = fmaf(wt, r[G * D + G + h], L);
    o0 = fmaf(wt, a[0], o0);
    o1 = fmaf(wt, a[1], o1);
  }
  const float inv = 1.f / L;
  __hip_bfloat162 r2 = __float22bfloat162_rn(make_float2(o0 * inv, o1 * inv));
  *reinterpret_cast<__hip_bfloat162*>(out + ((size_t)bh * G + h) * D + d0) = r2;
}

extern "C" void kernel_launch(void* const* d_in, const int* in_sizes, int n_in, void* d_out, int out_size,
                              void* d_ws, size_t ws_size, hipStream_t stream) {
  (void)in_sizes; (void)n_in; (void)out_size; (void)ws_size;
  (void)hipFuncSetAttribute((const void*)attn_split, hipFuncAttributeMaxDynamicSharedMemorySize, LDS_BYTES);
  attn_split<<<dim3(B_ * HKV * NSPLIT), dim3(WAVES * 64), LDS_BYTES, stream>>>(
      (const char*)d_in[0], (const char*)d_in[1], (const char*)d_in[2], (__hip_bfloat16*)d_out, (float*)d_ws);
  if (NSPLIT > 1) attn_combine<<<dim3(B_ * HKV), dim3(256), 0, stream>>>((const float*)d_ws, (__hip_bfloat16*)d_out);
}
```

```cpp
#include <hip/hip_runtime.h>
#include <hip/hip_bf16.h>
#include <stdint.h>

#define WAVES 4
#define PP 2
#define BAR 1
#define PACE 880
#define PACE0X 2400
#define ROT 0
#define KLATE 0
#define KPOL "nt"
#define VPOL "nt"

constexpr int B_ = 16, HQ = 32, HKV = 8, SK = 4096, D = 128, G = HQ / HKV;
constexpr int TILE = 16;
constexpr int KT = TILE * D * 2;
constexpr int VT = KT / 2;
constexpr int NT = SK / WAVES / TILE;
constexpr int RING = PP * (KT + VT);
constexpr int LDS_USED = WAVES * RING;
constexpr int LDS_BYTES = LDS_USED > 84 * 1024 ? LDS_USED : 84 * 1024;
constexpr float C2 = 0.08838834764831845f * 1.4426950408889634f;
static_assert(PP >= 1 && PP <= 4, "ring depth");
static_assert(RING >= G * 64 * 4 + 32, "merge record fits the ring");

typedef __attribute__((ext_vector_type(8))) __bf16 bf16x8;
typedef __attribute__((ext_vector_type(2))) __bf16 bf16x2;
typedef __attribute__((ext_vector_type(4))) short s16x4;
typedef __attribute__((ext_vector_type(4))) float f32x4;
typedef __attribute__((ext_vector_type(2))) float f32x2;
typedef __attribute__((ext_vector_type(4))) unsigned u32x4;
typedef __attribute__((ext_vector_type(2))) unsigned u32x2;

__device__ __forceinline__ void dma_k(unsigned lds, const char* src, unsigned v0, unsigned v1, unsigned v2, unsigned v3) {
  unsigned keep;
  asm volatile(
      "s_mov_b32 %0, m0\n\t"
      "s_mov_b32 m0, %1\n\t"
      "s_nop 0\n\t"
      "global_load_lds_dwordx4 %3, %2 offset:0 " KPOL "\n\t"
      "global_load_lds_dwordx4 %4, %2 offset:1024 " KPOL "\n\t"
      "global_load_lds_dwordx4 %5, %2 offset:2048 " KPOL "\n\t"
      "global_load_lds_dwordx4 %6, %2 offset:3072 " KPOL "\n\t"
      "s_mov_b32 m0, %0"
      : "=&s"(keep)
      : "s"(lds), "s"(src), "v"(v0), "v"(v1), "v"(v2), "v"(v3)
      : "memory");
}
__device__ __forceinline__ void dma_v(unsigned lds, const char* src, unsigned v0, unsigned v1) {
  unsigned keep;
  asm volatile(
      "s_mov_b32 %0, m0\n\t"
      "s_mov_b32 m0, %1\n\t"
      "s_nop 0\n\t"
      "global_load_lds_dwordx4 %3, %2 offset:0 " VPOL "\n\t"
      "global_load_lds_dwordx4 %4, %2 offset:1024 " VPOL "\n\t"
      "s_mov_b32 m0, %0"
      : "=&s"(keep)
      : "s"(lds), "s"(src), "v"(v0), "v"(v1)
      : "memory");
}

__device__ __forceinline__ float xrow_max(float x) {
  auto s = __builtin_amdgcn_permlane16_swap(__float_as_uint(x), __float_as_uint(x), false, false);
  x = fmaxf(__uint_as_float(s[0]), __uint_as_float(s[1]));
  auto t = __builtin_amdgcn_permlane32_swap(__float_as_uint(x), __float_as_uint(x), false, false);
  return fmaxf(__uint_as_float(t[0]), __uint_as_float(t[1]));
}
__device__ __forceinline__ float xrow_sum(float x) {
  auto s = __builtin_amdgcn_permlane16_swap(__float_as_uint(x), __float_as_uint(x), false, false);
  x = __uint_as_float(s[0]) + __uint_as_float(s[1]);
  auto t = __builtin_amdgcn_permlane32_swap(__float_as_uint(x), __float_as_uint(x), false, false);
  return __uint_as_float(t[0]) + __uint_as_float(t[1]);
}

__global__ void __launch_bounds__(WAVES * 64, 2)
attn_dsplit(const char* __restrict__ Q, const char* __restrict__ K, const char* __restrict__ V,
            __hip_bfloat16* __restrict__ out, float* __restrict__ ws) {
  extern __shared__ __attribute__((aligned(16))) char lds[];
  const int tid = threadIdx.x, lane = tid & 63, wave = __builtin_amdgcn_readfirstlane(tid >> 6);
  const int bh = (blockIdx.x >> 4) * 8 + (blockIdx.x & 7), half = (blockIdx.x >> 3) & 1;
  const int g = lane >> 4, t = lane & 15;
  const unsigned ring = (unsigned)(size_t)lds + wave * RING;
  const unsigned tstart = (unsigned)__builtin_amdgcn_s_memrealtime();
  unsigned kv[4], vv[2];
#pragma unroll
  for (int i = 0; i < 4; ++i) kv[i] = 256 * g + 16 * (t ^ g ^ (4 * i));
  {
    const int r = lane >> 3, c = lane & 7;
    vv[0] = 256 * r + 16 * (c ^ (2 * ((r >> 1) & 3)));
    vv[1] = vv[0] + 1024;
  }
  const size_t base = ((size_t)bh * SK + (size_t)wave * TILE) * (D * 2);
  const char* kp = K + base;
  const char* vp = V + base + half * 128;
  u32x4 q0, q1, q2, q3;
  {
    const __amdgpu_buffer_rsrc_t qr = __builtin_amdgcn_make_buffer_rsrc((void*)(Q + (size_t)bh * (G * D * 2)), (short)0, G * D * 2, 0x00020000);
    const unsigned qoff = t * 256 + g * 16;
    asm volatile(
        "s_nop 4\n\t"
        "buffer_load_dwordx4 %0, %4, %5, 0 offen offset:0\n\t"
        "buffer_load_dwordx4 %1, %4, %5, 0 offen offset:64\n\t"
        "buffer_load_dwordx4 %2, %4, %5, 0 offen offset:128\n\t"
        "buffer_load_dwordx4 %3, %4, %5, 0 offen offset:192"
        : "=&v"(q0), "=&v"(q1), "=&v"(q2), "=&v"(q3)
        : "v"(qoff), "s"(qr)
        : "memory");
  }
  constexpr unsigned STEP = WAVES * KT;
  const unsigned rot = ((unsigned)bh * ROT) & (NT - 1);
#pragma unroll
  for (int p = 0; p < PP; ++p) {
    const unsigned off = ((rot + p) & (NT - 1)) * STEP;
    dma_k(ring + p * KT, kp + off, kv[0], kv[1], kv[2], kv[3]);
    dma_v(ring + PP * KT + p * VT, vp + off, vv[0], vv[1]);
  }
  unsigned ka[4], va[4];
#pragma unroll
  for (int i = 0; i < 4; ++i) ka[i] = ring + ((256 * t + 16 * (g ^ t)) ^ (64 * i));
  {
    const int row = 4 * g + (t >> 2);
    const unsigned a0 = 128 * row + 32 * ((row >> 1) & 3) + 8 * (t & 3);
#pragma unroll
    for (int c = 0; c < 4; ++c) va[c] = ring + PP * KT + (a0 ^ (32 * c));
  }
  f32x4 o[4];
#pragma unroll
  for (int c = 0; c < 4; ++c) o[c] = f32x4{0.f, 0.f, 0.f, 0.f};
  float m = -INFINITY, l = 0.f;
  unsigned kslot = ring, vslot = ring + PP * KT;
  int slot = 0;
  asm volatile("s_waitcnt vmcnt(%4)" : "+v"(q0), "+v"(q1), "+v"(q2), "+v"(q3) : "n"(6 * PP) : "memory");

#pragma clang loop unroll(disable)
  for (int it = 0; it < NT; ++it) {
    const bool more = it + PP < NT;
    const int left = NT - it < PP ? NT - it : PP;
    if (left == PP) asm volatile("s_waitcnt vmcnt(%0)" ::"n"(6 * PP - 4) : "memory");
    else if (PP > 3 && left == 3) asm volatile("s_waitcnt vmcnt(14)" ::: "memory");
    else if (PP > 2 && left == 2) asm volatile("s_waitcnt vmcnt(8)" ::: "memory");
    else asm volatile("s_waitcnt vmcnt(2)" ::: "memory");
    u32x4 k0, k1, k2, k3;
    asm volatile(
        "ds_read_b128 %0, %4\n\t"
        "ds_read_b128 %1, %5\n\t"
        "ds_read_b128 %2, %6\n\t"
        "ds_read_b128 %3, %7\n\t"
        "s_waitcnt lgkmcnt(0)"
        : "=&v"(k0), "=&v"(k1), "=&v"(k2), "=&v"(k3)
        : "v"(ka[0]), "v"(ka[1]), "v"(ka[2]), "v"(ka[3])
        : "memory");
    const unsigned noff = ((rot + it + PP) & (NT - 1)) * STEP;
    if (!KLATE && more) dma_k(kslot, kp + noff, kv[0], kv[1], kv[2], kv[3]);
    f32x4 sc = {0.f, 0.f, 0.f, 0.f};
    sc = __builtin_amdgcn_mfma_f32_16x16x32_bf16(__builtin_bit_cast(bf16x8, k0), __builtin_bit_cast(bf16x8, q0), sc, 0, 0, 0);
    sc = __builtin_amdgcn_mfma_f32_16x16x32_bf16(__builtin_bit_cast(bf16x8, k1), __builtin_bit_cast(bf16x8, q1), sc, 0, 0, 0);
    sc = __builtin_amdgcn_mfma_f32_16x16x32_bf16(__builtin_bit_cast(bf16x8, k2), __builtin_bit_cast(bf16x8, q2), sc, 0, 0, 0);
    sc = __builtin_amdgcn_mfma_f32_16x16x32_bf16(__builtin_bit_cast(bf16x8, k3), __builtin_bit_cast(bf16x8, q3), sc, 0, 0, 0);
    const float tmax = xrow_max(fmaxf(fmaxf(sc[0], sc[1]), fmaxf(sc[2], sc[3])));
    const float mn = fmaxf(m, tmax);
    if (__any(mn > m)) {
      const float alpha = __builtin_amdgcn_exp2f((m - mn) * C2);
      l *= alpha;
#pragma unroll
      for (int c = 0; c < 4; ++c) o[c] *= alpha;
      m = mn;
    }
    const float mc = -m * C2;
    const f32x2 pa = {__builtin_amdgcn_exp2f(fmaf(sc[0], C2, mc)), __builtin_amdgcn_exp2f(fmaf(sc[1], C2, mc))};
    const f32x2 pb = {__builtin_amdgcn_exp2f(fmaf(sc[2], C2, mc)), __builtin_amdgcn_exp2f(fmaf(sc[3], C2, mc))};
    l += (pa[0] + pa[1]) + (pb[0] + pb[1]);
    const u32x2 pk = {__builtin_bit_cast(unsigned, __builtin_convertvector(pa, bf16x2)), __builtin_bit_cast(unsigned, __builtin_convertvector(pb, bf16x2))};
    const s16x4 pB = __builtin_bit_cast(s16x4, pk);
    if (!KLATE && more) asm volatile("s_waitcnt vmcnt(%0)" ::"n"(6 * PP - 2) : "memory");
    else if (left == PP && PP > 1) asm volatile("s_waitcnt vmcnt(%0)" ::"n"(6 * PP - 6) : "memory");
    else if (PP > 3 && left == 3) asm volatile("s_waitcnt vmcnt(12)" ::: "memory");
    else if (PP > 2 && left == 2) asm volatile("s_waitcnt vmcnt(6)" ::: "memory");
    else asm volatile("s_waitcnt vmcnt(0)" ::: "memory");
    u32x2 v0, v1, v2, v3;
    asm volatile(
        "ds_read_b64_tr_b16 %0, %4\n\t"
        "ds_read_b64_tr_b16 %1, %5\n\t"
        "ds_read_b64_tr_b16 %2, %6\n\t"
        "ds_read_b64_tr_b16 %3, %7\n\t"
        "s_waitcnt lgkmcnt(0)"
        : "=&v"(v0), "=&v"(v1), "=&v"(v2), "=&v"(v3)
        : "v"(va[0]), "v"(va[1]), "v"(va[2]), "v"(va[3])
        : "memory");
    if (PACE && more && wave == 0) {
      const unsigned due = (unsigned)(it * PACE + PACE0X) >> 4;
      while ((unsigned)__builtin_amdgcn_s_memrealtime() - tstart < due) __builtin_amdgcn_s_sleep(1);
    }
    if (BAR && more) __builtin_amdgcn_s_barrier();
    if (KLATE && more) dma_k(kslot, kp + noff, kv[0], kv[1], kv[2], kv[3]);
    if (more) dma_v(vslot, vp + noff, vv[0], vv[1]);
#define PV(c, v) o[c] = __builtin_amdgcn_mfma_f32_16x16x16bf16_1k(__builtin_bit_cast(s16x4, v), pB, o[c], 0, 0, 0)
    PV(0, v0); PV(1, v1); PV(2, v2); PV(3, v3);
#undef PV
    if (PP > 1) {
      const bool wrap = ++slot == PP;
      if (wrap) slot = 0;
      const unsigned dk = wrap ? -(unsigned)((PP - 1) * KT) : (unsigned)KT, dv = wrap ? -(unsigned)((PP - 1) * VT) : (unsigned)VT;
      kslot += dk; vslot += dv;
#pragma unroll
      for (int i = 0; i < 4; ++i) { ka[i] += dk; va[i] += dv; }
    }
  }

  const float lsum = xrow_sum(l);
  float* mine = reinterpret_cast<float*>(lds + wave * RING);
  if (t < G) {
#pragma unroll
    for (int c = 0; c < 4; ++c) *reinterpret_cast<f32x4*>(mine + t * 64 + 16 * c + 4 * g) = o[c];
    if (g == 0) { mine[G * 64 + t] = m; mine[G * 64 + G + t] = lsum; }
  }
  __syncthreads();
  if (tid < 128) {
    const int h = tid >> 5, d0 = 2 * (tid & 31);
    float M = -INFINITY;
#pragma unroll
    for (int w = 0; w < WAVES; ++w) M = fmaxf(M, reinterpret_cast<const float*>(lds + w * RING)[G * 64 + h]);
    float L = 0.f, o0 = 0.f, o1 = 0.f;
#pragma unroll
    for (int w = 0; w < WAVES; ++w) {
      const float* r = reinterpret_cast<const float*>(lds + w * RING);
      const float wt = __builtin_amdgcn_exp2f((r[G * 64 + h] - M) * C2);
      const f32x2 a = *reinterpret_cast<const f32x2*>(r + h * 64 + d0);
      L = fmaf(wt, r[G * 64 + G + h], L);
      o0 = fmaf(wt, a[0], o0);
      o1 = fmaf(wt, a[1], o1);
    }
    const float inv = __builtin_amdgcn_rcpf(L);
    const f32x2 res = {o0 * inv, o1 * inv};
    const unsigned packed = __builtin_bit_cast(unsigned, __builtin_convertvector(res, bf16x2));
    __hip_bfloat16* dst = out + ((size_t)bh * G + h) * D + half * 64 + d0;
    *reinterpret_cast<unsigned*>(dst) = packed;
  }
}

extern "C" void kernel_launch(void* const* d_in, const int* in_sizes, int n_in, void* d_out, int out_size,
                              void* d_ws, size_t ws_size, hipStream_t stream) {
  (void)in_sizes; (void)n_in; (void)out_size; (void)ws_size;
  (void)hipFuncSetAttribute((const void*)attn_dsplit, hipFuncAttributeMaxDynamicSharedMemorySize, LDS_BYTES);
  attn_dsplit<<<dim3(B_ * HKV * 2), dim3(WAVES * 64), LDS_BYTES, stream>>>(
      (const char*)d_in[0], (const char*)d_in[1], (const char*)d_in[2], (__hip_bfloat16*)d_out, (float*)d_ws);
}
```

```cpp
#include <hip/hip_runtime.h>
#include <hip/hip_bf16.h>
#include <stdint.h>

#define WAVES 4
#define PP 2
#define BAR 1
#define PACE 880
#define PACE0X 2200
#define ROT 0
#define KLATE 1
#define LATEM 25
#define KPOL "nt"
#define VPOL "nt"

constexpr int B_ = 16, HQ = 32, HKV = 8, SK = 4096, D = 128, G = HQ / HKV;
constexpr int TILE = 16;
constexpr int KT = TILE * D * 2;
constexpr int VT = KT / 2;
constexpr int NT = SK / WAVES / TILE;
constexpr int RING = PP * (KT + VT);
constexpr int LDS_USED = WAVES * RING;
constexpr int LDS_BYTES = LDS_USED > 84 * 1024 ? LDS_USED : 84 * 1024;
constexpr float C2 = 0.08838834764831845f * 1.4426950408889634f;
static_assert(PP >= 1 && PP <= 4, "ring depth");
static_assert(RING >= G * 64 * 4 + 32, "merge record fits the ring");

typedef __attribute__((ext_vector_type(8))) __bf16 bf16x8;
typedef __attribute__((ext_vector_type(2))) __bf16 bf16x2;
typedef __attribute__((ext_vector_type(4))) short s16x4;
typedef __attribute__((ext_vector_type(4))) float f32x4;
typedef __attribute__((ext_vector_type(2))) float f32x2;
typedef __attribute__((ext_vector_type(4))) unsigned u32x4;
typedef __attribute__((ext_vector_type(2))) unsigned u32x2;

__device__ __forceinline__ void dma_k(unsigned lds, const char* src, unsigned v0, unsigned v1, unsigned v2, unsigned v3) {
  unsigned keep;
  asm volatile(
      "s_mov_b32 %0, m0\n\t"
      "s_mov_b32 m0, %1\n\t"
      "s_nop 0\n\t"
      "global_load_lds_dwordx4 %3, %2 offset:0 " KPOL "\n\t"
      "global_load_lds_dwordx4 %4, %2 offset:1024 " KPOL "\n\t"
      "global_load_lds_dwordx4 %5, %2 offset:2048 " KPOL "\n\t"
      "global_load_lds_dwordx4 %6, %2 offset:3072 " KPOL "\n\t"
      "s_mov_b32 m0, %0"
      : "=&s"(keep)
      : "s"(lds), "s"(src), "v"(v0), "v"(v1), "v"(v2), "v"(v3)
      : "memory");
}
__device__ __forceinline__ void dma_v(unsigned lds, const char* src, unsigned v0, unsigned v1) {
  unsigned keep;
  asm volatile(
      "s_mov_b32 %0, m0\n\t"
      "s_mov_b32 m0, %1\n\t"
      "s_nop 0\n\t"
      "global_load_lds_dwordx4 %3, %2 offset:0 " VPOL "\n\t"
      "global_load_lds_dwordx4 %4, %2 offset:1024 " VPOL "\n\t"
      "s_mov_b32 m0, %0"
      : "=&s"(keep)
      : "s"(lds), "s"(src), "v"(v0), "v"(v1)
      : "memory");
}

__device__ __forceinline__ float xrow_max(float x) {
  auto s = __builtin_amdgcn_permlane16_swap(__float_as_uint(x), __float_as_uint(x), false, false);
  x = fmaxf(__uint_as_float(s[0]), __uint_as_float(s[1]));
  auto t = __builtin_amdgcn_permlane32_swap(__float_as_uint(x), __float_as_uint(x), false, false);
  return fmaxf(__uint_as_float(t[0]), __uint_as_float(t[1]));
}
__device__ __forceinline__ float xrow_sum(float x) {
  auto s = __builtin_amdgcn_permlane16_swap(__float_as_uint(x), __float_as_uint(x), false, false);
  x = __uint_as_float(s[0]) + __uint_as_float(s[1]);
  auto t = __builtin_amdgcn_permlane32_swap(__float_as_uint(x), __float_as_uint(x), false, false);
  return __uint_as_float(t[0]) + __uint_as_float(t[1]);
}

__global__ void __launch_bounds__(WAVES * 64, 2)
attn_dsplit(const char* __restrict__ Q, const char* __restrict__ K, const char* __restrict__ V,
            __hip_bfloat16* __restrict__ out, float* __restrict__ ws) {
  extern __shared__ __attribute__((aligned(16))) char lds[];
  const int tid = threadIdx.x, lane = tid & 63, wave = __builtin_amdgcn_readfirstlane(tid >> 6);
  const int bh = (blockIdx.x >> 4) * 8 + (blockIdx.x & 7), half = (blockIdx.x >> 3) & 1;
  const int g = lane >> 4, t = lane & 15;
  const unsigned ring = (unsigned)(size_t)lds + wave * RING;
  const unsigned tstart = (unsigned)__builtin_amdgcn_s_memrealtime();
  unsigned kv[4], vv[2];
#pragma unroll
  for (int i = 0; i < 4; ++i) kv[i] = 256 * g + 16 * (t ^ g ^ (4 * i));
  {
    const int r = lane >> 3, c = lane & 7;
    vv[0] = 256 * r + 16 * (c ^ (2 * ((r >> 1) & 3)));
    vv[1] = vv[0] + 1024;
  }
  const size_t base = ((size_t)bh * SK + (size_t)wave * TILE) * (D * 2);
  const char* kp = K + base;
  const char* vp = V + base + half * 128;
  u32x4 q0, q1, q2, q3;
  {
    const __amdgpu_buffer_rsrc_t qr = __builtin_amdgcn_make_buffer_rsrc((void*)(Q + (size_t)bh * (G * D * 2)), (short)0, G * D * 2, 0x00020000);
    const unsigned qoff = t * 256 + g * 16;
    asm volatile(
        "s_nop 4\n\t"
        "buffer_load_dwordx4 %0, %4, %5, 0 offen offset:0\n\t"
        "buffer_load_dwordx4 %1, %4, %5, 0 offen offset:64\n\t"
        "buffer_load_dwordx4 %2, %4, %5, 0 offen offset:128\n\t"
        "buffer_load_dwordx4 %3, %4, %5, 0 offen offset:192"
        : "=&v"(q0), "=&v"(q1), "=&v"(q2), "=&v"(q3)
        : "v"(qoff), "s"(qr)
        : "memory");
  }
  constexpr unsigned STEP = WAVES * KT;
  const unsigned rot = ((unsigned)bh * ROT) & (NT - 1);
#pragma unroll
  for (int p = 0; p < PP; ++p) {
    const unsigned off = ((rot + p) & (NT - 1)) * STEP;
    dma_k(ring + p * KT, kp + off, kv[0], kv[1], kv[2], kv[3]);
    dma_v(ring + PP * KT + p * VT, vp + off, vv[0], vv[1]);
  }
  unsigned ka[4], va[4];
#pragma unroll
  for (int i = 0; i < 4; ++i) ka[i] = ring + ((256 * t + 16 * (g ^ t)) ^ (64 * i));
  {
    const int row = 4 * g + (t >> 2);
    const unsigned a0 = 128 * row + 32 * ((row >> 1) & 3) + 8 * (t & 3);
#pragma unroll
    for (int c = 0; c < 4; ++c) va[c] = ring + PP * KT + (a0 ^ (32 * c));
  }
  f32x4 o[4];
#pragma unroll
  for (int c = 0; c < 4; ++c) o[c] = f32x4{0.f, 0.f, 0.f, 0.f};
  float m = -INFINITY, l = 0.f;
  unsigned kslot = ring, vslot = ring + PP * KT;
  int slot = 0;
  asm volatile("s_waitcnt vmcnt(%4)" : "+v"(q0), "+v"(q1), "+v"(q2), "+v"(q3) : "n"(6 * PP) : "memory");

#pragma clang loop unroll(disable)
  for (int it = 0; it < NT; ++it) {
    const bool more = it + PP < NT;
    const int left = NT - it < PP ? NT - it : PP;
    bool kearly = KLATE == 0;
    if (KLATE == 2) kearly = (int)((unsigned)__builtin_amdgcn_s_memrealtime() - tstart) <= (((it - 1) * PACE + PACE0X) >> 4) + LATEM;
    if (left == PP) asm volatile("s_waitcnt vmcnt(%0)" ::"n"(6 * PP - 4) : "memory");
    else if (PP > 3 && left == 3) asm volatile("s_waitcnt vmcnt(14)" ::: "memory");
    else if (PP > 2 && left == 2) asm volatile("s_waitcnt vmcnt(8)" ::: "memory");
    else asm volatile("s_waitcnt vmcnt(2)" ::: "memory");
    u32x4 k0, k1, k2, k3;
    asm volatile(
        "ds_read_b128 %0, %4\n\t"
        "ds_read_b128 %1, %5\n\t"
        "ds_read_b128 %2, %6\n\t"
        "ds_read_b128 %3, %7\n\t"
        "s_waitcnt lgkmcnt(0)"
        : "=&v"(k0), "=&v"(k1), "=&v"(k2), "=&v"(k3)
        : "v"(ka[0]), "v"(ka[1]), "v"(ka[2]), "v"(ka[3])
        : "memory");
    const unsigned noff = ((rot + it + PP) & (NT - 1)) * STEP;
    if (kearly && more) dma_k(kslot, kp + noff, kv[0], kv[1], kv[2], kv[3]);
    f32x4 sc = {0.f, 0.f, 0.f, 0.f};
    sc = __builtin_amdgcn_mfma_f32_16x16x32_bf16(__builtin_bit_cast(bf16x8, k0), __builtin_bit_cast(bf16x8, q0), sc, 0, 0, 0);
    sc = __builtin_amdgcn_mfma_f32_16x16x32_bf16(__builtin_bit_cast(bf16x8, k1), __builtin_bit_cast(bf16x8, q1), sc, 0, 0, 0);
    sc = __builtin_amdgcn_mfma_f32_16x16x32_bf16(__builtin_bit_cast(bf16x8, k2), __builtin_bit_cast(bf16x8, q2), sc, 0, 0, 0);
    sc = __builtin_amdgcn_mfma_f32_16x16x32_bf16(__builtin_bit_cast(bf16x8, k3), __builtin_bit_cast(bf16x8, q3), sc, 0, 0, 0);
    const float tmax = xrow_max(fmaxf(fmaxf(sc[0], sc[1]), fmaxf(sc[2], sc[3])));
    const float mn = fmaxf(m, tmax);
    if (__any(mn > m)) {
      const float alpha = __builtin_amdgcn_exp2f((m - mn) * C2);
      l *= alpha;
#pragma unroll
      for (int c = 0; c < 4; ++c) o[c] *= alpha;
      m = mn;
    }
    const float mc = -m * C2;
    const f32x2 pa = {__builtin_amdgcn_exp2f(fmaf(sc[0], C2, mc)), __builtin_amdgcn_exp2f(fmaf(sc[1], C2, mc))};
    const f32x2 pb = {__builtin_amdgcn_exp2f(fmaf(sc[2], C2, mc)), __builtin_amdgcn_exp2f(fmaf(sc[3], C2, mc))};
    l += (pa[0] + pa[1]) + (pb[0] + pb[1]);
    const u32x2 pk = {__builtin_bit_cast(unsigned, __builtin_convertvector(pa, bf16x2)), __builtin_bit_cast(unsigned, __builtin_convertvector(pb, bf16x2))};
    const s16x4 pB = __builtin_bit_cast(s16x4, pk);
    if (kearly && more) asm volatile("s_waitcnt vmcnt(%0)" ::"n"(6 * PP - 2) : "memory");
    else if (left == PP && PP > 1) asm volatile("s_waitcnt vmcnt(%0)" ::"n"(6 * PP - 6) : "memory");
    else if (PP > 3 && left == 3) asm volatile("s_waitcnt vmcnt(12)" ::: "memory");
    else if (PP > 2 && left == 2) asm volatile("s_waitcnt vmcnt(6)" ::: "memory");
    else asm volatile("s_waitcnt vmcnt(0)" ::: "memory");
    u32x2 v0, v1, v2, v3;
    asm volatile(
        "ds_read_b64_tr_b16 %0, %4\n\t"
        "ds_read_b64_tr_b16 %1, %5\n\t"
        "ds_read_b64_tr_b16 %2, %6\n\t"
        "ds_read_b64_tr_b16 %3, %7\n\t"
        "s_waitcnt lgkmcnt(0)"
        : "=&v"(v0), "=&v"(v1), "=&v"(v2), "=&v"(v3)
        : "v"(va[0]), "v"(va[1]), "v"(va[2]), "v"(va[3])
        : "memory");
    if (PACE && more && wave == 0) {
      const unsigned due = (unsigned)(it * PACE + PACE0X) >> 4;
      while ((unsigned)__builtin_amdgcn_s_memrealtime() - tstart < due) __builtin_amdgcn_s_sleep(1);
    }
    if (BAR && more) __builtin_amdgcn_s_barrier();
    if (!kearly && more) dma_k(kslot, kp + noff, kv[0], kv[1], kv[2], kv[3]);
    if (more) dma_v(vslot, vp + noff, vv[0], vv[1]);
#define PV(c, v) o[c] = __builtin_amdgcn_mfma_f32_16x16x16bf16_1k(__builtin_bit_cast(s16x4, v), pB, o[c], 0, 0, 0)
    PV(0, v0); PV(1, v1); PV(2, v2); PV(3, v3);
#undef PV
    if (PP > 1) {
      const bool wrap = ++slot == PP;
      if (wrap) slot = 0;
      const unsigned dk = wrap ? -(unsigned)((PP - 1) * KT) : (unsigned)KT, dv = wrap ? -(unsigned)((PP - 1) * VT) : (unsigned)VT;
      kslot += dk; vslot += dv;
#pragma unroll
      for (int i = 0; i < 4; ++i) { ka[i] += dk; va[i] += dv; }
    }
  }

  const float lsum = xrow_sum(l);
  float* mine = reinterpret_cast<float*>(lds + wave * RING);
  if (t < G) {
#pragma unroll
    for (int c = 0; c < 4; ++c) *reinterpret_cast<f32x4*>(mine + t * 64 + 16 * c + 4 * g) = o[c];
    if (g == 0) { mine[G * 64 + t] = m; mine[G * 64 + G + t] = lsum; }
  }
  __syncthreads();
  if (tid < 128) {
    const int h = tid >> 5, d0 = 2 * (tid & 31);
    float M = -INFINITY;
#pragma unroll
    for (int w = 0; w < WAVES; ++w) M = fmaxf(M, reinterpret_cast<const float*>(lds + w * RING)[G * 64 + h]);
    float L = 0.f, o0 = 0.f, o1 = 0.f;
#pragma unroll
    for (int w = 0; w < WAVES; ++w) {
      const float* r = reinterpret_cast<const float*>(lds + w * RING);
      const float wt = __builtin_amdgcn_exp2f((r[G * 64 + h] - M) * C2);
      const f32x2 a = *reinterpret_cast<const f32x2*>(r + h * 64 + d0);
      L = fmaf(wt, r[G * 64 + G + h], L);
      o0 = fmaf(wt, a[0], o0);
      o1 = fmaf(wt, a[1], o1);
    }
    const float inv = __builtin_amdgcn_rcpf(L);
    const f32x2 res = {o0 * inv, o1 * inv};
    const unsigned packed = __builtin_bit_cast(unsigned, __builtin_convertvector(res, bf16x2));
    __hip_bfloat16* dst = out + ((size_t)bh * G + h) * D + half * 64 + d0;
    *reinterpret_cast<unsigned*>(dst) = packed;
  }
}

extern "C" void kernel_launch(void* const* d_in, const int* in_sizes, int n_in, void* d_out, int out_size,
                              void* d_ws, size_t ws_size, hipStream_t stream) {
  (void)in_sizes; (void)n_in; (void)out_size; (void)ws_size;
  (void)hipFuncSetAttribute((const void*)attn_dsplit, hipFuncAttributeMaxDynamicSharedMemorySize, LDS_BYTES);
  attn_dsplit<<<dim3(B_ * HKV * 2), dim3(WAVES * 64), LDS_BYTES, stream>>>(
      (const char*)d_in[0], (const char*)d_in[1], (const char*)d_in[2], (__hip_bfloat16*)d_out, (float*)d_ws);
}
```

```cpp
#include <hip/hip_runtime.h>
#include <hip/hip_bf16.h>
#include <stdint.h>

#define WAVES 4
#define PP 2
#define BAR 1
#define PACE 880
#define PACE0X 2200
#define ROT 0
#define KLATE 0
#define LATEM 25
#define KPOL "nt"
#define VPOL "nt"

constexpr int B_ = 16, HQ = 32, HKV = 8, SK = 4096, D = 128, G = HQ / HKV;
constexpr int TILE = 16;
constexpr int KT = TILE * D * 2;
constexpr int VT = KT / 2;
constexpr int NT = SK / WAVES / TILE;
constexpr int RING = PP * (KT + VT);
constexpr int LDS_USED = WAVES * RING;
constexpr int LDS_BYTES = LDS_USED > 84 * 1024 ? LDS_USED : 84 * 1024;
constexpr float C2 = 0.08838834764831845f * 1.4426950408889634f;
static_assert(PP >= 1 && PP <= 4, "ring depth");
static_assert(RING >= G * 64 * 4 + 32, "merge record fits the ring");

typedef __attribute__((ext_vector_type(8))) __bf16 bf16x8;
typedef __attribute__((ext_vector_type(2))) __bf16 bf16x2;
typedef __attribute__((ext_vector_type(4))) short s16x4;
typedef __attribute__((ext_vector_type(4))) float f32x4;
typedef __attribute__((ext_vector_type(2))) float f32x2;
typedef __attribute__((ext_vector_type(4))) unsigned u32x4;
typedef __attribute__((ext_vector_type(2))) unsigned u32x2;

__device__ __forceinline__ void dma_k(unsigned lds, const char* src, unsigned v0, unsigned v1, unsigned v2, unsigned v3) {
  unsigned keep;
  asm volatile(
      "s_mov_b32 %0, m0\n\t"
      "s_mov_b32 m0, %1\n\t"
      "s_nop 0\n\t"
      "global_load_lds_dwordx4 %3, %2 offset:0 " KPOL "\n\t"
      "global_load_lds_dwordx4 %4, %2 offset:1024 " KPOL "\n\t"
      "global_load_lds_dwordx4 %5, %2 offset:2048 " KPOL "\n\t"
      "global_load_lds_dwordx4 %6, %2 offset:3072 " KPOL "\n\t"
      "s_mov_b32 m0, %0"
      : "=&s"(keep)
      : "s"(lds), "s"(src), "v"(v0), "v"(v1), "v"(v2), "v"(v3)
      : "memory");
}
__device__ __forceinline__ void dma_v(unsigned lds, const char* src, unsigned v0, unsigned v1) {
  unsigned keep;
  asm volatile(
      "s_mov_b32 %0, m0\n\t"
      "s_mov_b32 m0, %1\n\t"
      "s_nop 0\n\t"
      "global_load_lds_dwordx4 %3, %2 offset:0 " VPOL "\n\t"
      "global_load_lds_dwordx4 %4, %2 offset:1024 " VPOL "\n\t"
      "s_mov_b32 m0, %0"
      : "=&s"(keep)
      : "s"(lds), "s"(src), "v"(v0), "v"(v1)
      : "memory");
}

__device__ __forceinline__ float xrow_max(float x) {
  auto s = __builtin_amdgcn_permlane16_swap(__float_as_uint(x), __float_as_uint(x), false, false);
  x = fmaxf(__uint_as_float(s[0]), __uint_as_float(s[1]));
  auto t = __builtin_amdgcn_permlane32_swap(__float_as_uint(x), __float_as_uint(x), false, false);
  return fmaxf(__uint_as_float(t[0]), __uint_as_float(t[1]));
}
__device__ __forceinline__ float xrow_sum(float x) {
  auto s = __builtin_amdgcn_permlane16_swap(__float_as_uint(x), __float_as_uint(x), false, false);
  x = __uint_as_float(s[0]) + __uint_as_float(s[1]);
  auto t = __builtin_amdgcn_permlane32_swap(__float_as_uint(x), __float_as_uint(x), false, false);
  return __uint_as_float(t[0]) + __uint_as_float(t[1]);
}

__global__ void __launch_bounds__(WAVES * 64, 2)
attn_dsplit(const char* __restrict__ Q, const char* __restrict__ K, const char* __restrict__ V,
            __hip_bfloat16* __restrict__ out, float* __restrict__ ws) {
  extern __shared__ __attribute__((aligned(16))) char lds[];
  const int tid = threadIdx.x, lane = tid & 63, wave = __builtin_amdgcn_readfirstlane(tid >> 6);
  const int bh = (blockIdx.x >> 4) * 8 + (blockIdx.x & 7), half = (blockIdx.x >> 3) & 1;
  const int g = lane >> 4, t = lane & 15;
  const unsigned ring = (unsigned)(size_t)lds + wave * RING;
  const unsigned tstart = (unsigned)__builtin_amdgcn_s_memrealtime();
  unsigned kv[4], vv[2];
#pragma unroll
  for (int i = 0; i < 4; ++i) kv[i] = 256 * g + 16 * (t ^ g ^ (4 * i));
  {
    const int r = lane >> 3, c = lane & 7;
    vv[0] = 256 * r + 16 * (c ^ (2 * ((r >> 1) & 3)));
    vv[1] = vv[0] + 1024;
  }
  const size_t base = ((size_t)bh * SK + (size_t)wave * TILE) * (D * 2);
  const char* kp = K + base;
  const char* vp = V + base + half * 128;
  u32x4 q0, q1, q2, q3;
  {
    const __amdgpu_buffer_rsrc_t qr = __builtin_amdgcn_make_buffer_rsrc((void*)(Q + (size_t)bh * (G * D * 2)), (short)0, G * D * 2, 0x00020000);
    const unsigned qoff = t * 256 + g * 16;
    asm volatile(
        "s_nop 4\n\t"
        "buffer_load_dwordx4 %0, %4, %5, 0 offen offset:0\n\t"
        "buffer_load_dwordx4 %1, %4, %5, 0 offen offset:64\n\t"
        "buffer_load_dwordx4 %2, %4, %5, 0 offen offset:128\n\t"
        "buffer_load_dwordx4 %3, %4, %5, 0 offen offset:192"
        : "=&v"(q0), "=&v"(q1), "=&v"(q2), "=&v"(q3)
        : "v"(qoff), "s"(qr)
        : "memory");
  }
  constexpr unsigned STEP = WAVES * KT;
  const unsigned rot = ((unsigned)bh * ROT) & (NT - 1);
#pragma unroll
  for (int p = 0; p < PP; ++p) {
    const unsigned off = ((rot + p) & (NT - 1)) * STEP;
    dma_k(ring + p * KT, kp + off, kv[0], kv[1], kv[2], kv[3]);
    dma_v(ring + PP * KT + p * VT, vp + off, vv[0], vv[1]);
  }
  unsigned ka[4], va[4];
#pragma unroll
  for (int i = 0; i < 4; ++i) ka[i] = ring + ((256 * t + 16 * (g ^ t)) ^ (64 * i));
  {
    const int row = 4 * g + (t >> 2);
    const unsigned a0 = 128 * row + 32 * ((row >> 1) & 3) + 8 * (t & 3);
#pragma unroll
    for (int c = 0; c < 4; ++c) va[c] = ring + PP * KT + (a0 ^ (32 * c));
  }
  f32x4 o[4];
#pragma unroll
  for (int c = 0; c < 4; ++c) o[c] = f32x4{0.f, 0.f, 0.f, 0.f};
  float m = -INFINITY, l = 0.f;
  unsigned kslot = ring, vslot = ring + PP * KT;
  int slot = 0;
  asm volatile("s_waitcnt vmcnt(%4)" : "+v"(q0), "+v"(q1), "+v"(q2), "+v"(q3) : "n"(6 * PP) : "memory");

#pragma clang loop unroll(disable)
  for (int it = 0; it < NT; ++it) {
    const bool more = it + PP < NT;
    const int left = NT - it < PP ? NT - it : PP;
    if (left == PP && PP > 1) asm volatile("s_waitcnt vmcnt(%0)" ::"n"(6 * PP - 6) : "memory");
    else if (PP > 3 && left == 3) asm volatile("s_waitcnt vmcnt(12)" ::: "memory");
    else if (PP > 2 && left == 2) asm volatile("s_waitcnt vmcnt(6)" ::: "memory");
    else asm volatile("s_waitcnt vmcnt(0)" ::: "memory");
    u32x4 k0, k1, k2, k3;
    u32x2 v0, v1, v2, v3;
    asm volatile(
        "ds_read_b128 %0, %8\n\t"
        "ds_read_b128 %1, %9\n\t"
        "ds_read_b128 %2, %10\n\t"
        "ds_read_b128 %3, %11\n\t"
        "ds_read_b64_tr_b16 %4, %12\n\t"
        "ds_read_b64_tr_b16 %5, %13\n\t"
        "ds_read_b64_tr_b16 %6, %14\n\t"
        "ds_read_b64_tr_b16 %7, %15\n\t"
        "s_waitcnt lgkmcnt(0)"
        : "=&v"(k0), "=&v"(k1), "=&v"(k2), "=&v"(k3), "=&v"(v0), "=&v"(v1), "=&v"(v2), "=&v"(v3)
        : "v"(ka[0]), "v"(ka[1]), "v"(ka[2]), "v"(ka[3]), "v"(va[0]), "v"(va[1]), "v"(va[2]), "v"(va[3])
        : "memory");
    if (more) {
      if (PACE && wave == 0) {
        const unsigned due = (unsigned)(it * PACE + PACE0X) >> 4;
        while ((unsigned)__builtin_amdgcn_s_memrealtime() - tstart < due) __builtin_amdgcn_s_sleep(1);
      }
      if (BAR) __builtin_amdgcn_s_barrier();
      const unsigned noff = ((rot + it + PP) & (NT - 1)) * STEP;
      dma_k(kslot, kp + noff, kv[0], kv[1], kv[2], kv[3]);
      dma_v(vslot, vp + noff, vv[0], vv[1]);
    }
    f32x4 sc = {0.f, 0.f, 0.f, 0.f};
    sc = __builtin_amdgcn_mfma_f32_16x16x32_bf16(__builtin_bit_cast(bf16x8, k0), __builtin_bit_cast(bf16x8, q0), sc, 0, 0, 0);
    sc = __builtin_amdgcn_mfma_f32_16x16x32_bf16(__builtin_bit_cast(bf16x8, k1), __builtin_bit_cast(bf16x8, q1), sc, 0, 0, 0);
    sc = __builtin_amdgcn_mfma_f32_16x16x32_bf16(__builtin_bit_cast(bf16x8, k2), __builtin_bit_cast(bf16x8, q2), sc, 0, 0, 0);
    sc = __builtin_amdgcn_mfma_f32_16x16x32_bf16(__builtin_bit_cast(bf16x8, k3), __builtin_bit_cast(bf16x8, q3), sc, 0, 0, 0);
    const float tmax = xrow_max(fmaxf(fmaxf(sc[0], sc[1]), fmaxf(sc[2], sc[3])));
    const float mn = fmaxf(m, tmax);
    if (__any(mn > m)) {
      const float alpha = __builtin_amdgcn_exp2f((m - mn) * C2);
      l *= alpha;
#pragma unroll
      for (int c = 0; c < 4; ++c) o[c] *= alpha;
      m = mn;
    }
    const float mc = -m * C2;
    const f32x2 pa = {__builtin_amdgcn_exp2f(fmaf(sc[0], C2, mc)), __builtin_amdgcn_exp2f(fmaf(sc[1], C2, mc))};
    const f32x2 pb = {__builtin_amdgcn_exp2f(fmaf(sc[2], C2, mc)), __builtin_amdgcn_exp2f(fmaf(sc[3], C2, mc))};
    l += (pa[0] + pa[1]) + (pb[0] + pb[1]);
    const u32x2 pk = {__builtin_bit_cast(unsigned, __builtin_convertvector(pa, bf16x2)), __builtin_bit_cast(unsigned, __builtin_convertvector(pb, bf16x2))};
    const s16x4 pB = __builtin_bit_cast(s16x4, pk);
#define PV(c, v) o[c] = __builtin_amdgcn_mfma_f32_16x16x16bf16_1k(__builtin_bit_cast(s16x4, v), pB, o[c], 0, 0, 0)
    PV(0, v0); PV(1, v1); PV(2, v2); PV(3, v3);
#undef PV
    if (PP > 1) {
      const bool wrap = ++slot == PP;
      if (wrap) slot = 0;
      const unsigned dk = wrap ? -(unsigned)((PP - 1) * KT) : (unsigned)KT, dv = wrap ? -(unsigned)((PP - 1) * VT) : (unsigned)VT;
      kslot += dk; vslot += dv;
#pragma unroll
      for (int i = 0; i < 4; ++i) { ka[i] += dk; va[i] += dv; }
    }
  }

  const float lsum = xrow_sum(l);
  float* mine = reinterpret_cast<float*>(lds + wave * RING);
  if (t < G) {
#pragma unroll
    for (int c = 0; c < 4; ++c) *reinterpret_cast<f32x4*>(mine + t * 64 + 16 * c + 4 * g) = o[c];
    if (g == 0) { mine[G * 64 + t] = m; mine[G * 64 + G + t] = lsum; }
  }
  __syncthreads();
  if (tid < 128) {
    const int h = tid >> 5, d0 = 2 * (tid & 31);
    float M = -INFINITY;
#pragma unroll
    for (int w = 0; w < WAVES; ++w) M = fmaxf(M, reinterpret_cast<const float*>(lds + w * RING)[G * 64 + h]);
    float L = 0.f, o0 = 0.f, o1 = 0.f;
#pragma unroll
    for (int w = 0; w < WAVES; ++w) {
      const float* r = reinterpret_cast<const float*>(lds + w * RING);
      const float wt = __builtin_amdgcn_exp2f((r[G * 64 + h] - M) * C2);
      const f32x2 a = *reinterpret_cast<const f32x2*>(r + h * 64 + d0);
      L = fmaf(wt, r[G * 64 + G + h], L);
      o0 = fmaf(wt, a[0], o0);
      o1 = fmaf(wt, a[1], o1);
    }
    const float inv = __builtin_amdgcn_rcpf(L);
    const f32x2 res = {o0 * inv, o1 * inv};
    const unsigned packed = __builtin_bit_cast(unsigned, __builtin_convertvector(res, bf16x2));
    __hip_bfloat16* dst = out + ((size_t)bh * G + h) * D + half * 64 + d0;
    *reinterpret_cast<unsigned*>(dst) = packed;
  }
}

extern "C" void kernel_launch(void* const* d_in, const int* in_sizes, int n_in, void* d_out, int out_size,
                              void* d_ws, size_t ws_size, hipStream_t stream) {
  (void)in_sizes; (void)n_in; (void)out_size; (void)ws_size;
  (void)hipFuncSetAttribute((const void*)attn_dsplit, hipFuncAttributeMaxDynamicSharedMemorySize, LDS_BYTES);
  attn_dsplit<<<dim3(B_ * HKV * 2), dim3(WAVES * 64), LDS_BYTES, stream>>>(
      (const char*)d_in[0], (const char*)d_in[1], (const char*)d_in[2], (__hip_bfloat16*)d_out, (float*)d_ws);
}
```

```cpp
#include <hip/hip_runtime.h>
#include <hip/hip_bf16.h>
#include <stdint.h>

#define WAVES 4
#define PP 3
#define BAR 1
#define PACE 880
#define PACE0X 1800
#define ROT 0
#define KLATE 0
#define LATEM 25
#define P0 2
#define KPOL "nt"
#define VPOL "nt"

constexpr int B_ = 16, HQ = 32, HKV = 8, SK = 4096, D = 128, G = HQ / HKV;
constexpr int TILE = 16;
constexpr int KT = TILE * D * 2;
constexpr int VT = KT / 2;
constexpr int NT = SK / WAVES / TILE;
constexpr int RING = PP * (KT + VT);
constexpr int LDS_USED = WAVES * RING;
constexpr int LDS_BYTES = LDS_USED > 84 * 1024 ? LDS_USED : 84 * 1024;
constexpr float C2 = 0.08838834764831845f * 1.4426950408889634f;
static_assert(PP >= 1 && PP <= 4, "ring depth");
static_assert(RING >= G * 64 * 4 + 32, "merge record fits the ring");

typedef __attribute__((ext_vector_type(8))) __bf16 bf16x8;
typedef __attribute__((ext_vector_type(2))) __bf16 bf16x2;
typedef __attribute__((ext_vector_type(4))) short s16x4;
typedef __attribute__((ext_vector_type(4))) float f32x4;
typedef __attribute__((ext_vector_type(2))) float f32x2;
typedef __attribute__((ext_vector_type(4))) unsigned u32x4;
typedef __attribute__((ext_vector_type(2))) unsigned u32x2;

__device__ __forceinline__ void dma_k(unsigned lds, const char* src, unsigned v0, unsigned v1, unsigned v2, unsigned v3) {
  unsigned keep;
  asm volatile(
      "s_mov_b32 %0, m0\n\t"
      "s_mov_b32 m0, %1\n\t"
      "s_nop 0\n\t"
      "global_load_lds_dwordx4 %3, %2 offset:0 " KPOL "\n\t"
      "global_load_lds_dwordx4 %4, %2 offset:1024 " KPOL "\n\t"
      "global_load_lds_dwordx4 %5, %2 offset:2048 " KPOL "\n\t"
      "global_load_lds_dwordx4 %6, %2 offset:3072 " KPOL "\n\t"
      "s_mov_b32 m0, %0"
      : "=&s"(keep)
      : "s"(lds), "s"(src), "v"(v0), "v"(v1), "v"(v2), "v"(v3)
      : "memory");
}
__device__ __forceinline__ void dma_v(unsigned lds, const char* src, unsigned v0, unsigned v1) {
  unsigned keep;
  asm volatile(
      "s_mov_b32 %0, m0\n\t"
      "s_mov_b32 m0, %1\n\t"
      "s_nop 0\n\t"
      "global_load_lds_dwordx4 %3, %2 offset:0 " VPOL "\n\t"
      "global_load_lds_dwordx4 %4, %2 offset:1024 " VPOL "\n\t"
      "s_mov_b32 m0, %0"
      : "=&s"(keep)
      : "s"(lds), "s"(src), "v"(v0), "v"(v1)
      : "memory");
}

__device__ __forceinline__ float xrow_max(float x) {
  auto s = __builtin_amdgcn_permlane16_swap(__float_as_uint(x), __float_as_uint(x), false, false);
  x = fmaxf(__uint_as_float(s[0]), __uint_as_float(s[1]));
  auto t = __builtin_amdgcn_permlane32_swap(__float_as_uint(x), __float_as_uint(x), false, false);
  return fmaxf(__uint_as_float(t[0]), __uint_as_float(t[1]));
}
__device__ __forceinline__ float xrow_sum(float x) {
  auto s = __builtin_amdgcn_permlane16_swap(__float_as_uint(x), __float_as_uint(x), false, false);
  x = __uint_as_float(s[0]) + __uint_as_float(s[1]);
  auto t = __builtin_amdgcn_permlane32_swap(__float_as_uint(x), __float_as_uint(x), false, false);
  return __uint_as_float(t[0]) + __uint_as_float(t[1]);
}

__global__ void __launch_bounds__(WAVES * 64, 2)
attn_dsplit(const char* __restrict__ Q, const char* __restrict__ K, const char* __restrict__ V,
            __hip_bfloat16* __restrict__ out, float* __restrict__ ws) {
  extern __shared__ __attribute__((aligned(16))) char lds[];
  const int tid = threadIdx.x, lane = tid & 63, wave = __builtin_amdgcn_readfirstlane(tid >> 6);
  const int bh = (blockIdx.x >> 4) * 8 + (blockIdx.x & 7), half = (blockIdx.x >> 3) & 1;
  const int g = lane >> 4, t = lane & 15;
  const unsigned ring = (unsigned)(size_t)lds + wave * RING;
  const unsigned tstart = (unsigned)__builtin_amdgcn_s_memrealtime();
  unsigned kv[4], vv[2];
#pragma unroll
  for (int i = 0; i < 4; ++i) kv[i] = 256 * g + 16 * (t ^ g ^ (4 * i));
  {
    const int r = lane >> 3, c = lane & 7;
    vv[0] = 256 * r + 16 * (c ^ (2 * ((r >> 1) & 3)));
    vv[1] = vv[0] + 1024;
  }
  const size_t base = ((size_t)bh * SK + (size_t)wave * TILE) * (D * 2);
  const char* kp = K + base;
  const char* vp = V + base + half * 128;
  u32x4 q0, q1, q2, q3;
  {
    const __amdgpu_buffer_rsrc_t qr = __builtin_amdgcn_make_buffer_rsrc((void*)(Q + (size_t)bh * (G * D * 2)), (short)0, G * D * 2, 0x00020000);
    const unsigned qoff = t * 256 + g * 16;
    asm volatile(
        "s_nop 4\n\t"
        "buffer_load_dwordx4 %0, %4, %5, 0 offen offset:0\n\t"
        "buffer_load_dwordx4 %1, %4, %5, 0 offen offset:64\n\t"
        "buffer_load_dwordx4 %2, %4, %5, 0 offen offset:128\n\t"
        "buffer_load_dwordx4 %3, %4, %5, 0 offen offset:192"
        : "=&v"(q0), "=&v"(q1), "=&v"(q2), "=&v"(q3)
        : "v"(qoff), "s"(qr)
        : "memory");
  }
  constexpr unsigned STEP = WAVES * KT;
  const unsigned rot = ((unsigned)bh * ROT) & (NT - 1);
#pragma unroll
  for (int p = 0; p < P0; ++p) {
    const unsigned off = ((rot + p) & (NT - 1)) * STEP;
    dma_k(ring + p * KT, kp + off, kv[0], kv[1], kv[2], kv[3]);
    dma_v(ring + PP * KT + p * VT, vp + off, vv[0], vv[1]);
  }
  unsigned ka[4], va[4];
#pragma unroll
  for (int i = 0; i < 4; ++i) ka[i] = ring + ((256 * t + 16 * (g ^ t)) ^ (64 * i));
  {
    const int row = 4 * g + (t >> 2);
    const unsigned a0 = 128 * row + 32 * ((row >> 1) & 3) + 8 * (t & 3);
#pragma unroll
    for (int c = 0; c < 4; ++c) va[c] = ring + PP * KT + (a0 ^ (32 * c));
  }
  f32x4 o[4];
#pragma unroll
  for (int c = 0; c < 4; ++c) o[c] = f32x4{0.f, 0.f, 0.f, 0.f};
  float m = -INFINITY, l = 0.f;
  unsigned kslot = ring, vslot = ring + PP * KT;
  int slot = 0;
  asm volatile("s_waitcnt vmcnt(%4)" : "+v"(q0), "+v"(q1), "+v"(q2), "+v"(q3) : "n"(6 * P0) : "memory");

#pragma clang loop unroll(disable)
  for (int it = 0; it < NT; ++it) {
    const bool more = it + PP < NT;
    const int left = it == 0 ? P0 : NT - it < PP ? NT - it : PP;
    if (left == PP && PP > 1) asm volatile("s_waitcnt vmcnt(%0)" ::"n"(6 * PP - 6) : "memory");
    else if (PP > 3 && left == 3) asm volatile("s_waitcnt vmcnt(12)" ::: "memory");
    else if (PP > 2 && left == 2) asm volatile("s_waitcnt vmcnt(6)" ::: "memory");
    else asm volatile("s_waitcnt vmcnt(0)" ::: "memory");
    u32x4 k0, k1, k2, k3;
    u32x2 v0, v1, v2, v3;
    asm volatile(
        "ds_read_b128 %0, %8\n\t"
        "ds_read_b128 %1, %9\n\t"
        "ds_read_b128 %2, %10\n\t"
        "ds_read_b128 %3, %11\n\t"
        "ds_read_b64_tr_b16 %4, %12\n\t"
        "ds_read_b64_tr_b16 %5, %13\n\t"
        "ds_read_b64_tr_b16 %6, %14\n\t"
        "ds_read_b64_tr_b16 %7, %15\n\t"
        "s_waitcnt lgkmcnt(0)"
        : "=&v"(k0), "=&v"(k1), "=&v"(k2), "=&v"(k3), "=&v"(v0), "=&v"(v1), "=&v"(v2), "=&v"(v3)
        : "v"(ka[0]), "v"(ka[1]), "v"(ka[2]), "v"(ka[3]), "v"(va[0]), "v"(va[1]), "v"(va[2]), "v"(va[3])
        : "memory");
    if (more) {
      if (PACE && wave == 0) {
        const unsigned due = (unsigned)(it * PACE + PACE0X) >> 4;
        while ((unsigned)__builtin_amdgcn_s_memrealtime() - tstart < due) __builtin_amdgcn_s_sleep(1);
      }
      if (BAR) __builtin_amdgcn_s_barrier();
      if (P0 < PP && it == 0) {
#pragma unroll
        for (int p = P0; p < PP; ++p) {
          dma_k(ring + p * KT, kp + ((rot + p) & (NT - 1)) * STEP, kv[0], kv[1], kv[2], kv[3]);
          dma_v(ring + PP * KT + p * VT, vp + ((rot + p) & (NT - 1)) * STEP, vv[0], vv[1]);
        }
      }
      const unsigned noff = ((rot + it + PP) & (NT - 1)) * STEP;
      dma_k(kslot, kp + noff, kv[0], kv[1], kv[2], kv[3]);
      dma_v(vslot, vp + noff, vv[0], vv[1]);
    }
    f32x4 sc = {0.f, 0.f, 0.f, 0.f};
    sc = __builtin_amdgcn_mfma_f32_16x16x32_bf16(__builtin_bit_cast(bf16x8, k0), __builtin_bit_cast(bf16x8, q0), sc, 0, 0, 0);
    sc = __builtin_amdgcn_mfma_f32_16x16x32_bf16(__builtin_bit_cast(bf16x8, k1), __builtin_bit_cast(bf16x8, q1), sc, 0, 0, 0);
    sc = __builtin_amdgcn_mfma_f32_16x16x32_bf16(__builtin_bit_cast(bf16x8, k2), __builtin_bit_cast(bf16x8, q2), sc, 0, 0, 0);
    sc = __builtin_amdgcn_mfma_f32_16x16x32_bf16(__builtin_bit_cast(bf16x8, k3), __builtin_bit_cast(bf16x8, q3), sc, 0, 0, 0);
    const float tmax = xrow_max(fmaxf(fmaxf(sc[0], sc[1]), fmaxf(sc[2], sc[3])));
    const float mn = fmaxf(m, tmax);
    if (__any(mn > m)) {
      const float alpha = __builtin_amdgcn_exp2f((m - mn) * C2);
      l *= alpha;
#pragma unroll
      for (int c = 0; c < 4; ++c) o[c] *= alpha;
      m = mn;
    }
    const float mc = -m * C2;
    const f32x2 pa = {__builtin_amdgcn_exp2f(fmaf(sc[0], C2, mc)), __builtin_amdgcn_exp2f(fmaf(sc[1], C2, mc))};
    const f32x2 pb = {__builtin_amdgcn_exp2f(fmaf(sc[2], C2, mc)), __builtin_amdgcn_exp2f(fmaf(sc[3], C2, mc))};
    l += (pa[0] + pa[1]) + (pb[0] + pb[1]);
    const u32x2 pk = {__builtin_bit_cast(unsigned, __builtin_convertvector(pa, bf16x2)), __builtin_bit_cast(unsigned, __builtin_convertvector(pb, bf16x2))};
    const s16x4 pB = __builtin_bit_cast(s16x4, pk);
#define PV(c, v) o[c] = __builtin_amdgcn_mfma_f32_16x16x16bf16_1k(__builtin_bit_cast(s16x4, v), pB, o[c], 0, 0, 0)
    PV(0, v0); PV(1, v1); PV(2, v2); PV(3, v3);
#undef PV
    if (PP > 1) {
      const bool wrap = ++slot == PP;
      if (wrap) slot = 0;
      const unsigned dk = wrap ? -(unsigned)((PP - 1) * KT) : (unsigned)KT, dv = wrap ? -(unsigned)((PP - 1) * VT) : (unsigned)VT;
      kslot += dk; vslot += dv;
#pragma unroll
      for (int i = 0; i < 4; ++i) { ka[i] += dk; va[i] += dv; }
    }
  }

  const float lsum = xrow_sum(l);
  float* mine = reinterpret_cast<float*>(lds + wave * RING);
  if (t < G) {
#pragma unroll
    for (int c = 0; c < 4; ++c) *reinterpret_cast<f32x4*>(mine + t * 64 + 16 * c + 4 * g) = o[c];
    if (g == 0) { mine[G * 64 + t] = m; mine[G * 64 + G + t] = lsum; }
  }
  __syncthreads();
  if (tid < 128) {
    const int h = tid >> 5, d0 = 2 * (tid & 31);
    float M = -INFINITY;
#pragma unroll
    for (int w = 0; w < WAVES; ++w) M = fmaxf(M, reinterpret_cast<const float*>(lds + w * RING)[G * 64 + h]);
    float L = 0.f, o0 = 0.f, o1 = 0.f;
#pragma unroll
    for (int w = 0; w < WAVES; ++w) {
      const float* r = reinterpret_cast<const float*>(lds + w * RING);
      const float wt = __builtin_amdgcn_exp2f((r[G * 64 + h] - M) * C2);
      const f32x2 a = *reinterpret_cast<const f32x2*>(r + h * 64 + d0);
      L = fmaf(wt, r[G * 64 + G + h], L);
      o0 = fmaf(wt, a[0], o0);
      o1 = fmaf(wt, a[1], o1);
    }
    const float inv = __builtin_amdgcn_rcpf(L);
    const f32x2 res = {o0 * inv, o1 * inv};
    const unsigned packed = __builtin_bit_cast(unsigned, __builtin_convertvector(res, bf16x2));
    __hip_bfloat16* dst = out + ((size_t)bh * G + h) * D + half * 64 + d0;
    *reinterpret_cast<unsigned*>(dst) = packed;
  }
}

extern "C" void kernel_launch(void* const* d_in, const int* in_sizes, int n_in, void* d_out, int out_size,
                              void* d_ws, size_t ws_size, hipStream_t stream) {
  (void)in_sizes; (void)n_in; (void)out_size; (void)ws_size;
  (void)hipFuncSetAttribute((const void*)attn_dsplit, hipFuncAttributeMaxDynamicSharedMemorySize, LDS_BYTES);
  attn_dsplit<<<dim3(B_ * HKV * 2), dim3(WAVES * 64), LDS_BYTES, stream>>>(
      (const char*)d_in[0], (const char*)d_in[1], (const char*)d_in[2], (__hip_bfloat16*)d_out, (float*)d_ws);
}
```

```cpp
#include <hip/hip_runtime.h>
#include <hip/hip_bf16.h>
#include <stdint.h>

#define WAVES 4
#define DEPTH 2
#define PP (DEPTH + 1)
#define BAR 1
#define PACE 880
#define PACE0X 2000
#define ROT 0
#define KLATE 0
#define LATEM 25
#define KPOL "nt"
#define VPOL "nt"

constexpr int B_ = 16, HQ = 32, HKV = 8, SK = 4096, D = 128, G = HQ / HKV;
constexpr int TILE = 16;
constexpr int KT = TILE * D * 2;
constexpr int VT = KT / 2;
constexpr int NT = SK / WAVES / TILE;
constexpr int RING = PP * (KT + VT);
constexpr int LDS_USED = WAVES * RING;
constexpr int LDS_BYTES = LDS_USED > 84 * 1024 ? LDS_USED : 84 * 1024;
constexpr float C2 = 0.08838834764831845f * 1.4426950408889634f;
static_assert(PP >= 1 && PP <= 4, "ring depth");
static_assert(RING >= G * 64 * 4 + 32, "merge record fits the ring");

typedef __attribute__((ext_vector_type(8))) __bf16 bf16x8;
typedef __attribute__((ext_vector_type(2))) __bf16 bf16x2;
typedef __attribute__((ext_vector_type(4))) short s16x4;
typedef __attribute__((ext_vector_type(4))) float f32x4;
typedef __attribute__((ext_vector_type(2))) float f32x2;
typedef __attribute__((ext_vector_type(4))) unsigned u32x4;
typedef __attribute__((ext_vector_type(2))) unsigned u32x2;

__device__ __forceinline__ void dma_k(unsigned lds, const char* src, unsigned v0, unsigned v1, unsigned v2, unsigned v3) {
  unsigned keep;
  asm volatile(
      "s_mov_b32 %0, m0\n\t"
      "s_mov_b32 m0, %1\n\t"
      "s_nop 0\n\t"
      "global_load_lds_dwordx4 %3, %2 offset:0 " KPOL "\n\t"
      "global_load_lds_dwordx4 %4, %2 offset:1024 " KPOL "\n\t"
      "global_load_lds_dwordx4 %5, %2 offset:2048 " KPOL "\n\t"
      "global_load_lds_dwordx4 %6, %2 offset:3072 " KPOL "\n\t"
      "s_mov_b32 m0, %0"
      : "=&s"(keep)
      : "s"(lds), "s"(src), "v"(v0), "v"(v1), "v"(v2), "v"(v3)
      : "memory");
}
__device__ __forceinline__ void dma_v(unsigned lds, const char* src, unsigned v0, unsigned v1) {
  unsigned keep;
  asm volatile(
      "s_mov_b32 %0, m0\n\t"
      "s_mov_b32 m0, %1\n\t"
      "s_nop 0\n\t"
      "global_load_lds_dwordx4 %3, %2 offset:0 " VPOL "\n\t"
      "global_load_lds_dwordx4 %4, %2 offset:1024 " VPOL "\n\t"
      "s_mov_b32 m0, %0"
      : "=&s"(keep)
      : "s"(lds), "s"(src), "v"(v0), "v"(v1)
      : "memory");
}

__device__ __forceinline__ float xrow_max(float x) {
  auto s = __builtin_amdgcn_permlane16_swap(__float_as_uint(x), __float_as_uint(x), false, false);
  x = fmaxf(__uint_as_float(s[0]), __uint_as_float(s[1]));
  auto t = __builtin_amdgcn_permlane32_swap(__float_as_uint(x), __float_as_uint(x), false, false);
  return fmaxf(__uint_as_float(t[0]), __uint_as_float(t[1]));
}
__device__ __forceinline__ float xrow_sum(float x) {
  auto s = __builtin_amdgcn_permlane16_swap(__float_as_uint(x), __float_as_uint(x), false, false);
  x = __uint_as_float(s[0]) + __uint_as_float(s[1]);
  auto t = __builtin_amdgcn_permlane32_swap(__float_as_uint(x), __float_as_uint(x), false, false);
  return __uint_as_float(t[0]) + __uint_as_float(t[1]);
}

__global__ void __launch_bounds__(WAVES * 64, 2)
attn_dsplit(const char* __restrict__ Q, const char* __restrict__ K, const char* __restrict__ V,
            __hip_bfloat16* __restrict__ out, float* __restrict__ ws) {
  extern __shared__ __attribute__((aligned(16))) char lds[];
  const int tid = threadIdx.x, lane = tid & 63, wave = __builtin_amdgcn_readfirstlane(tid >> 6);
  const int bh = (blockIdx.x >> 4) * 8 + (blockIdx.x & 7), half = (blockIdx.x >> 3) & 1;
  const int g = lane >> 4, t = lane & 15;
  const unsigned ring = (unsigned)(size_t)lds + wave * RING;
  const unsigned tstart = (unsigned)__builtin_amdgcn_s_memrealtime();
  unsigned kv[4], vv[2];
#pragma unroll
  for (int i = 0; i < 4; ++i) kv[i] = 256 * g + 16 * (t ^ g ^ (4 * i));
  {
    const int r = lane >> 3, c = lane & 7;
    vv[0] = 256 * r + 16 * (c ^ (2 * ((r >> 1) & 3)));
    vv[1] = vv[0] + 1024;
  }
  const size_t base = ((size_t)bh * SK + (size_t)wave * TILE) * (D * 2);
  const char* kp = K + base;
  const char* vp = V + base + half * 128;
  u32x4 q0, q1, q2, q3;
  {
    const __amdgpu_buffer_rsrc_t qr = __builtin_amdgcn_make_buffer_rsrc((void*)(Q + (size_t)bh * (G * D * 2)), (short)0, G * D * 2, 0x00020000);
    const unsigned qoff = t * 256 + g * 16;
    asm volatile(
        "s_nop 4\n\t"
        "buffer_load_dwordx4 %0, %4, %5, 0 offen offset:0\n\t"
        "buffer_load_dwordx4 %1, %4, %5, 0 offen offset:64\n\t"
        "buffer_load_dwordx4 %2, %4, %5, 0 offen offset:128\n\t"
        "buffer_load_dwordx4 %3, %4, %5, 0 offen offset:192"
        : "=&v"(q0), "=&v"(q1), "=&v"(q2), "=&v"(q3)
        : "v"(qoff), "s"(qr)
        : "memory");
  }
  constexpr unsigned STEP = WAVES * KT;
  const unsigned rot = ((unsigned)bh * ROT) & (NT - 1);
#pragma unroll
  for (int p = 0; p < DEPTH; ++p) {
    const unsigned off = ((rot + p) & (NT - 1)) * STEP;
    dma_k(ring + p * KT, kp + off, kv[0], kv[1], kv[2], kv[3]);
    dma_v(ring + PP * KT + p * VT, vp + off, vv[0], vv[1]);
  }
  unsigned ka[4], va[4];
#pragma unroll
  for (int i = 0; i < 4; ++i) ka[i] = ring + ((256 * t + 16 * (g ^ t)) ^ (64 * i));
  {
    const int row = 4 * g + (t >> 2);
    const unsigned a0 = 128 * row + 32 * ((row >> 1) & 3) + 8 * (t & 3);
#pragma unroll
    for (int c = 0; c < 4; ++c) va[c] = ring + PP * KT + (a0 ^ (32 * c));
  }
  f32x4 o[4];
#pragma unroll
  for (int c = 0; c < 4; ++c) o[c] = f32x4{0.f, 0.f, 0.f, 0.f};
  float m = -INFINITY, l = 0.f;
  unsigned kslot = ring, vslot = ring + PP * KT;
  int slot = 0;
  asm volatile("s_waitcnt vmcnt(%4)" : "+v"(q0), "+v"(q1), "+v"(q2), "+v"(q3) : "n"(6 * DEPTH) : "memory");

  unsigned kfill = ring + DEPTH * KT, vfill = ring + PP * KT + DEPTH * VT;
#pragma clang loop unroll(disable)
  for (int it = 0; it < NT; ++it) {
    const bool more = it + DEPTH < NT;
    const int left = NT - it < DEPTH ? NT - it : DEPTH;
    if (left == DEPTH && DEPTH > 1) asm volatile("s_waitcnt vmcnt(%0)" ::"n"(6 * DEPTH - 6) : "memory");
    else if (DEPTH > 2 && left == 2) asm volatile("s_waitcnt vmcnt(6)" ::: "memory");
    else asm volatile("s_waitcnt vmcnt(0)" ::: "memory");
    if (more) {
      if (PACE && wave == 0) {
        const unsigned due = (unsigned)(it * PACE + PACE0X) >> 4;
        while ((unsigned)__builtin_amdgcn_s_memrealtime() - tstart < due) __builtin_amdgcn_s_sleep(1);
      }
      if (BAR) __builtin_amdgcn_s_barrier();
      const unsigned noff = ((rot + it + DEPTH) & (NT - 1)) * STEP;
      dma_k(kfill, kp + noff, kv[0], kv[1], kv[2], kv[3]);
      dma_v(vfill, vp + noff, vv[0], vv[1]);
    }
    u32x4 k0, k1, k2, k3;
    u32x2 v0, v1, v2, v3;
    asm volatile(
        "ds_read_b128 %0, %8\n\t"
        "ds_read_b128 %1, %9\n\t"
        "ds_read_b128 %2, %10\n\t"
        "ds_read_b128 %3, %11\n\t"
        "ds_read_b64_tr_b16 %4, %12\n\t"
        "ds_read_b64_tr_b16 %5, %13\n\t"
        "ds_read_b64_tr_b16 %6, %14\n\t"
        "ds_read_b64_tr_b16 %7, %15\n\t"
        "s_waitcnt lgkmcnt(0)"
        : "=&v"(k0), "=&v"(k1), "=&v"(k2), "=&v"(k3), "=&v"(v0), "=&v"(v1), "=&v"(v2), "=&v"(v3)
        : "v"(ka[0]), "v"(ka[1]), "v"(ka[2]), "v"(ka[3]), "v"(va[0]), "v"(va[1]), "v"(va[2]), "v"(va[3])
        : "memory");
    f32x4 sc = {0.f, 0.f, 0.f, 0.f};
    sc = __builtin_amdgcn_mfma_f32_16x16x32_bf16(__builtin_bit_cast(bf16x8, k0), __builtin_bit_cast(bf16x8, q0), sc, 0, 0, 0);
    sc = __builtin_amdgcn_mfma_f32_16x16x32_bf16(__builtin_bit_cast(bf16x8, k1), __builtin_bit_cast(bf16x8, q1), sc, 0, 0, 0);
    sc = __builtin_amdgcn_mfma_f32_16x16x32_bf16(__builtin_bit_cast(bf16x8, k2), __builtin_bit_cast(bf16x8, q2), sc, 0, 0, 0);
    sc = __builtin_amdgcn_mfma_f32_16x16x32_bf16(__builtin_bit_cast(bf16x8, k3), __builtin_bit_cast(bf16x8, q3), sc, 0, 0, 0);
    const float tmax = xrow_max(fmaxf(fmaxf(sc[0], sc[1]), fmaxf(sc[2], sc[3])));
    const float mn = fmaxf(m, tmax);
    if (__any(mn > m)) {
      const float alpha = __builtin_amdgcn_exp2f((m - mn) * C2);
      l *= alpha;
#pragma unroll
      for (int c = 0; c < 4; ++c) o[c] *= alpha;
      m = mn;
    }
    const float mc = -m * C2;
    const f32x2 pa = {__builtin_amdgcn_exp2f(fmaf(sc[0], C2, mc)), __builtin_amdgcn_exp2f(fmaf(sc[1], C2, mc))};
    const f32x2 pb = {__builtin_amdgcn_exp2f(fmaf(sc[2], C2, mc)), __builtin_amdgcn_exp2f(fmaf(sc[3], C2, mc))};
    l += (pa[0] + pa[1]) + (pb[0] + pb[1]);
    const u32x2 pk = {__builtin_bit_cast(unsigned, __builtin_convertvector(pa, bf16x2)), __builtin_bit_cast(unsigned, __builtin_convertvector(pb, bf16x2))};
    const s16x4 pB = __builtin_bit_cast(s16x4, pk);
#define PV(c, v) o[c] = __builtin_amdgcn_mfma_f32_16x16x16bf16_1k(__builtin_bit_cast(s16x4, v), pB, o[c], 0, 0, 0)
    PV(0, v0); PV(1, v1); PV(2, v2); PV(3, v3);
#undef PV
    {
      kfill = kslot; vfill = vslot;
      const bool wrap = ++slot == PP;
      if (wrap) slot = 0;
      const unsigned dk = wrap ? -(unsigned)((PP - 1) * KT) : (unsigned)KT, dv = wrap ? -(unsigned)((PP - 1) * VT) : (unsigned)VT;
      kslot += dk; vslot += dv;
#pragma unroll
      for (int i = 0; i < 4; ++i) { ka[i] += dk; va[i] += dv; }
    }
  }

  const float lsum = xrow_sum(l);
  float* mine = reinterpret_cast<float*>(lds + wave * RING);
  if (t < G) {
#pragma unroll
    for (int c = 0; c < 4; ++c) *reinterpret_cast<f32x4*>(mine + t * 64 + 16 * c + 4 * g) = o[c];
    if (g == 0) { mine[G * 64 + t] = m; mine[G * 64 + G + t] = lsum; }
  }
  __syncthreads();
  if (tid < 128) {
    const int h = tid >> 5, d0 = 2 * (tid & 31);
    float M = -INFINITY;
#pragma unroll
    for (int w = 0; w < WAVES; ++w) M = fmaxf(M, reinterpret_cast<const float*>(lds + w * RING)[G * 64 + h]);
    float L = 0.f, o0 = 0.f, o1 = 0.f;
#pragma unroll
    for (int w = 0; w < WAVES; ++w) {
      const float* r = reinterpret_cast<const float*>(lds + w * RING);
      const float wt = __builtin_amdgcn_exp2f((r[G * 64 + h] - M) * C2);
      const f32x2 a = *reinterpret_cast<const f32x2*>(r + h * 64 + d0);
      L = fmaf(wt, r[G * 64 + G + h], L);
      o0 = fmaf(wt, a[0], o0);
      o1 = fmaf(wt, a[1], o1);
    }
    const float inv = __builtin_amdgcn_rcpf(L);
    const f32x2 res = {o0 * inv, o1 * inv};
    const unsigned packed = __builtin_bit_cast(unsigned, __builtin_convertvector(res, bf16x2));
    __hip_bfloat16* dst = out + ((size_t)bh * G + h) * D + half * 64 + d0;
    *reinterpret_cast<unsigned*>(dst) = packed;
  }
}

extern "C" void kernel_launch(void* const* d_in, const int* in_sizes, int n_in, void* d_out, int out_size,
                              void* d_ws, size_t ws_size, hipStream_t stream) {
  (void)in_sizes; (void)n_in; (void)out_size; (void)ws_size;
  (void)hipFuncSetAttribute((const void*)attn_dsplit, hipFuncAttributeMaxDynamicSharedMemorySize, LDS_BYTES);
  attn_dsplit<<<dim3(B_ * HKV * 2), dim3(WAVES * 64), LDS_BYTES, stream>>>(
      (const char*)d_in[0], (const char*)d_in[1], (const char*)d_in[2], (__hip_bfloat16*)d_out, (float*)d_ws);
}
```
